```python
import torch

def _softmax(x, axis=-1):
    return torch.softmax(x, dim=axis)
def _gelu(x):
    return torch.nn.functional.gelu(x, approximate="tanh")
def _silu(x):
    return torch.nn.functional.silu(x)
def _sigmoid(x):
    return torch.sigmoid(x)
def _layernorm(x, axis=-1, eps=1e-5):
    m = x.mean(dim=axis, keepdim=True)
    v = x.var(dim=axis, keepdim=True, unbiased=False)
    return (x - m) / torch.sqrt(v + eps)
def _scatter_add(tgt, idx, src, axis=0):
    return tgt.clone().index_add_(axis, idx, src)
def _segment_sum(src, seg, n):
    out = torch.zeros((n,) + src.shape[1:], dtype=src.dtype)
    return out.index_add_(0, seg, src)
def _pad(x, pw):
    flat = tuple(v for pair in reversed(pw) for v in pair)
    return torch.nn.functional.pad(x, flat)

kernel_name = 'standalone__mpnn_edge_mlp__graph'

def setup_inputs():

    _w = (lambda a: (lambda t: t.to(torch.bfloat16) if t.is_floating_point() else t)(torch.as_tensor(a)))
    N, deg, D, De = 10000, 32, 128, max(16, 1)
    E = N * deg
    return {
        "x": _w((lambda *s: torch.randn(*s, dtype=torch.bfloat16) * 0.05)(N, D)),
        "e_attr": _w((lambda *s: torch.randn(*s, dtype=torch.bfloat16) * 0.4)(E, De)),
        "src": _w((lambda lo, hi, *s: torch.randint(lo, hi, s))(0, N, E).to(torch.int64)),
        "dst": _w((lambda lo, hi, *s: torch.randint(lo, hi, s))(0, N, E).to(torch.int64)),
        "w_msg": _w((lambda *s: torch.randn(*s, dtype=torch.bfloat16) * 0.4)(2 * D + De, D)),
        "w_upd": _w((lambda *s: torch.randn(*s, dtype=torch.bfloat16) * 0.05)(2 * D, D)),
    }

def reference(x, e_attr, src, dst, w_msg, w_upd):
    N, D = x.shape
    m_in = torch.concatenate([x[src], x[dst], e_attr], axis=-1)
    msg = _silu(m_in @ w_msg)
    agg = _segment_sum(msg, dst, N)
    return _silu(torch.concatenate([x, agg], axis=-1) @ w_upd)
```

```cpp
#include <hip/hip_runtime.h>
#include <hip/hip_bf16.h>

namespace {
using u32 = unsigned;
using u16 = unsigned short;
using u32x2 = __attribute__((ext_vector_type(2))) u32;
using u32x4 = __attribute__((ext_vector_type(4))) u32;
using u32x8 = __attribute__((ext_vector_type(8))) u32;
using u32x16 = __attribute__((ext_vector_type(16))) u32;
using f32x16 = __attribute__((ext_vector_type(16))) float;
#define DI __device__ __forceinline__
#define PIN(x) asm volatile("" : "+v"(x))

constexpr int NN = 10000, NE = 320000;
constexpr int CH = 128, CE = NE / CH;
constexpr int NB = 256, CAP = 32;
constexpr u32 BM = 26843;
constexpr int WROW = NN, TROWS = WROW + 128;

constexpr size_t WS_SYNC = 0;
constexpr u32 F_OCNT = 4096 + 256 * 128;
constexpr size_t WS_SLOT = 65536;
constexpr size_t WS_TAB = WS_SLOT + (size_t)NB * CH * CAP * 4;
constexpr size_t WS_W = WS_TAB + (size_t)TROWS * 256;
constexpr u32 W_BYTES = 32768;
constexpr size_t WS_OVF = WS_W + W_BYTES;

__device__ const u32 lane_tab[12 * 64 * 4] __attribute__((aligned(16))) = {
    0x0, 0x0, 0x0, 0x0, 0x0, 0x8, 0x0, 0x1, 0x0, 0x10, 0x0, 0x2, 0x0, 0x18, 0x0, 0x3, 0x1, 0x140, 0x0, 0x4, 0x1, 0x148, 0x0, 0x5, 0x1, 0x150, 0x0, 0x6, 0x1, 0x158, 0x0, 0x7,
    0x2, 0x280, 0x0, 0x8, 0x2, 0x288, 0x0, 0x9, 0x2, 0x290, 0x0, 0xa, 0x2, 0x298, 0x0, 0xb, 0x3, 0x3c0, 0x1, 0x0, 0x3, 0x3c8, 0x1, 0x1, 0x3, 0x3d0, 0x1, 0x2, 0x3, 0x3d8, 0x1, 0x3,
    0x4, 0x20, 0x1, 0x4, 0x4, 0x28, 0x1, 0x5, 0x4, 0x30, 0x1, 0x6, 0x4, 0x38, 0x1, 0x7, 0x5, 0x160, 0x1, 0x8, 0x5, 0x168, 0x1, 0x9, 0x5, 0x170, 0x1, 0xa, 0x5, 0x178, 0x1, 0xb,
    0x6, 0x2a0, 0x2, 0x0, 0x6, 0x2a8, 0x2, 0x1, 0x6, 0x2b0, 0x2, 0x2, 0x6, 0x2b8, 0x2, 0x3, 0x7, 0x3e0, 0x2, 0x4, 0x7, 0x3e8, 0x2, 0x5, 0x7, 0x3f0, 0x2, 0x6, 0x7, 0x3f8, 0x2, 0x7,
    0x8, 0x800, 0x2, 0x8, 0x8, 0x808, 0x2, 0x9, 0x8, 0x810, 0x2, 0xa, 0x8, 0x818, 0x2, 0xb, 0x9, 0x940, 0x3, 0x0, 0x9, 0x948, 0x3, 0x1, 0x9, 0x950, 0x3, 0x2, 0x9, 0x958, 0x3, 0x3,
    0xa, 0xa80, 0x3, 0x4, 0xa, 0xa88, 0x3, 0x5, 0xa, 0xa90, 0x3, 0x6, 0xa, 0xa98, 0x3, 0x7, 0xb, 0xbc0, 0x3, 0x8, 0xb, 0xbc8, 0x3, 0x9, 0xb, 0xbd0, 0x3, 0xa, 0xb, 0xbd8, 0x3, 0xb,
    0xc, 0x820, 0x4, 0x0, 0xc, 0x828, 0x4, 0x1, 0xc, 0x830, 0x4, 0x2, 0xc, 0x838, 0x4, 0x3, 0xd, 0x960, 0x4, 0x4, 0xd, 0x968, 0x4, 0x5, 0xd, 0x970, 0x4, 0x6, 0xd, 0x978, 0x4, 0x7,
    0xe, 0xaa0, 0x4, 0x8, 0xe, 0xaa8, 0x4, 0x9, 0xe, 0xab0, 0x4, 0xa, 0xe, 0xab8, 0x4, 0xb, 0xf, 0xbe0, 0x5, 0x7fff, 0xf, 0xbe8, 0x5, 0x7fff, 0xf, 0xbf0, 0x5, 0x7fff, 0xf, 0xbf8, 0x5, 0x7fff,
    0x0, 0x0, 0x2710, 0x0, 0x0, 0x2, 0x2711, 0x1, 0x0, 0x4, 0x2712, 0x2, 0x0, 0x6, 0x2713, 0x3, 0x0, 0x8, 0x2714, 0x4, 0x0, 0xa, 0x2715, 0x5, 0x0, 0xc, 0x2716, 0x6, 0x0, 0xe, 0x2717, 0x7,
    0x0, 0x10, 0x2718, 0x8, 0x0, 0x12, 0x2719, 0x9, 0x0, 0x14, 0x271a, 0xa, 0x0, 0x16, 0x271b, 0xb, 0x0, 0x18, 0x271c, 0xc, 0x0, 0x1a, 0x271d, 0xd, 0x0, 0x1c, 0x271e, 0xe, 0x0, 0x1e, 0x271f, 0xf,
    0x0, 0x20, 0x2720, 0x10, 0x0, 0x22, 0x2721, 0x11, 0x0, 0x24, 0x2722, 0x12, 0x0, 0x26, 0x2723, 0x13, 0x0, 0x28, 0x2724, 0x28, 0x0, 0x2a, 0x2725, 0x29, 0x0, 0x2c, 0x2726, 0x2a, 0x0, 0x2e, 0x2727, 0x2b,
    0x0, 0x30, 0x2728, 0x2c, 0x0, 0x32, 0x2729, 0x2d, 0x0, 0x34, 0x272a, 0x2e, 0x0, 0x36, 0x272b, 0x2f, 0x0, 0x38, 0x272c, 0x30, 0x0, 0x3a, 0x272d, 0x31, 0x0, 0x3c, 0x272e, 0x32, 0x0, 0x3e, 0x272f, 0x33,
    0x3c0, 0x80, 0x2730, 0x14, 0x3c0, 0x82, 0x2731, 0x15, 0x3c0, 0x84, 0x2732, 0x16, 0x3c0, 0x86, 0x2733, 0x17, 0x3c0, 0x88, 0x2734, 0x18, 0x3c0, 0x8a, 0x2735, 0x19, 0x3c0, 0x8c, 0x2736, 0x1a, 0x3c0, 0x8e, 0x2737, 0x1b,
    0x3c0, 0x90, 0x2738, 0x1c, 0x3c0, 0x92, 0x2739, 0x1d, 0x3c0, 0x94, 0x273a, 0x1e, 0x3c0, 0x96, 0x273b, 0x1f, 0x3c0, 0x98, 0x273c, 0x20, 0x3c0, 0x9a, 0x273d, 0x21, 0x3c0, 0x9c, 0x273e, 0x22, 0x3c0, 0x9e, 0x273f, 0x23,
    0x3c0, 0xa0, 0x2740, 0x24, 0x3c0, 0xa2, 0x2741, 0x25, 0x3c0, 0xa4, 0x2742, 0x26, 0x3c0, 0xa6, 0x2743, 0x27, 0x3c0, 0xa8, 0x2744, 0x34, 0x3c0, 0xaa, 0x2745, 0x35, 0x3c0, 0xac, 0x2746, 0x36, 0x3c0, 0xae, 0x2747, 0x37,
    0x3c0, 0xb0, 0x2748, 0x38, 0x3c0, 0xb2, 0x2749, 0x39, 0x3c0, 0xb4, 0x274a, 0x3a, 0x3c0, 0xb6, 0x274b, 0x3b, 0x3c0, 0xb8, 0x274c, 0x3c, 0x3c0, 0xba, 0x274d, 0x3d, 0x3c0, 0xbc, 0x274e, 0x3e, 0x3c0, 0xbe, 0x274f, 0x3f,
    0x0, 0xc00, 0x0, 0x840, 0x0, 0xc10, 0x0, 0x850, 0x0, 0xc20, 0x0, 0x860, 0x0, 0xc30, 0x0, 0x870, 0x0, 0xc40, 0x0, 0x800, 0x0, 0xc50, 0x0, 0x810, 0x0, 0xc60, 0x0, 0x820, 0x0, 0xc70, 0x0, 0x830,
    0x0, 0xc00, 0x0, 0x840, 0x0, 0xc10, 0x0, 0x850, 0x0, 0xc20, 0x0, 0x860, 0x0, 0xc30, 0x0, 0x870, 0x0, 0xc40, 0x0, 0x800, 0x0, 0xc50, 0x0, 0x810, 0x0, 0xc60, 0x0, 0x820, 0x0, 0xc70, 0x0, 0x830,
    0x0, 0xc10, 0x0, 0x850, 0x0, 0xc00, 0x0, 0x840, 0x0, 0xc30, 0x0, 0x870, 0x0, 0xc20, 0x0, 0x860, 0x0, 0xc50, 0x0, 0x810, 0x0, 0xc40, 0x0, 0x800, 0x0, 0xc70, 0x0, 0x830, 0x0, 0xc60, 0x0, 0x820,
    0x0, 0xc10, 0x0, 0x850, 0x0, 0xc00, 0x0, 0x840, 0x0, 0xc30, 0x0, 0x870, 0x0, 0xc20, 0x0, 0x860, 0x0, 0xc50, 0x0, 0x810, 0x0, 0xc40, 0x0, 0x800, 0x0, 0xc70, 0x0, 0x830, 0x0, 0xc60, 0x0, 0x820,
    0x0, 0xc20, 0x0, 0x860, 0x0, 0xc30, 0x0, 0x870, 0x0, 0xc00, 0x0, 0x840, 0x0, 0xc10, 0x0, 0x850, 0x0, 0xc60, 0x0, 0x820, 0x0, 0xc70, 0x0, 0x830, 0x0, 0xc40, 0x0, 0x800, 0x0, 0xc50, 0x0, 0x810,
    0x0, 0xc20, 0x0, 0x860, 0x0, 0xc30, 0x0, 0x870, 0x0, 0xc00, 0x0, 0x840, 0x0, 0xc10, 0x0, 0x850, 0x0, 0xc60, 0x0, 0x820, 0x0, 0xc70, 0x0, 0x830, 0x0, 0xc40, 0x0, 0x800, 0x0, 0xc50, 0x0, 0x810,
    0x0, 0xc30, 0x0, 0x870, 0x0, 0xc20, 0x0, 0x860, 0x0, 0xc10, 0x0, 0x850, 0x0, 0xc00, 0x0, 0x840, 0x0, 0xc70, 0x0, 0x830, 0x0, 0xc60, 0x0, 0x820, 0x0, 0xc50, 0x0, 0x810, 0x0, 0xc40, 0x0, 0x800,
    0x0, 0xc30, 0x0, 0x870, 0x0, 0xc20, 0x0, 0x860, 0x0, 0xc10, 0x0, 0x850, 0x0, 0xc00, 0x0, 0x840, 0x0, 0xc70, 0x0, 0x830, 0x0, 0xc60, 0x0, 0x820, 0x0, 0xc50, 0x0, 0x810, 0x0, 0xc40, 0x0, 0x800,
    0x0, 0x400, 0x0, 0x40, 0x0, 0x410, 0x0, 0x50, 0x0, 0x420, 0x0, 0x60, 0x0, 0x430, 0x0, 0x70, 0x0, 0x440, 0x0, 0x0, 0x0, 0x450, 0x0, 0x10, 0x0, 0x460, 0x0, 0x20, 0x0, 0x470, 0x0, 0x30,
    0x0, 0x400, 0x0, 0x40, 0x0, 0x410, 0x0, 0x50, 0x0, 0x420, 0x0, 0x60, 0x0, 0x430, 0x0, 0x70, 0x0, 0x440, 0x0, 0x0, 0x0, 0x450, 0x0, 0x10, 0x0, 0x460, 0x0, 0x20, 0x0, 0x470, 0x0, 0x30,
    0x0, 0x410, 0x0, 0x50, 0x0, 0x400, 0x0, 0x40, 0x0, 0x430, 0x0, 0x70, 0x0, 0x420, 0x0, 0x60, 0x0, 0x450, 0x0, 0x10, 0x0, 0x440, 0x0, 0x0, 0x0, 0x470, 0x0, 0x30, 0x0, 0x460, 0x0, 0x20,
    0x0, 0x410, 0x0, 0x50, 0x0, 0x400, 0x0, 0x40, 0x0, 0x430, 0x0, 0x70, 0x0, 0x420, 0x0, 0x60, 0x0, 0x450, 0x0, 0x10, 0x0, 0x440, 0x0, 0x0, 0x0, 0x470, 0x0, 0x30, 0x0, 0x460, 0x0, 0x20,
    0x0, 0x420, 0x0, 0x60, 0x0, 0x430, 0x0, 0x70, 0x0, 0x400, 0x0, 0x40, 0x0, 0x410, 0x0, 0x50, 0x0, 0x460, 0x0, 0x20, 0x0, 0x470, 0x0, 0x30, 0x0, 0x440, 0x0, 0x0, 0x0, 0x450, 0x0, 0x10,
    0x0, 0x420, 0x0, 0x60, 0x0, 0x430, 0x0, 0x70, 0x0, 0x400, 0x0, 0x40, 0x0, 0x410, 0x0, 0x50, 0x0, 0x460, 0x0, 0x20, 0x0, 0x470, 0x0, 0x30, 0x0, 0x440, 0x0, 0x0, 0x0, 0x450, 0x0, 0x10,
    0x0, 0x430, 0x0, 0x70, 0x0, 0x420, 0x0, 0x60, 0x0, 0x410, 0x0, 0x50, 0x0, 0x400, 0x0, 0x40, 0x0, 0x470, 0x0, 0x30, 0x0, 0x460, 0x0, 0x20, 0x0, 0x450, 0x0, 0x10, 0x0, 0x440, 0x0, 0x0,
    0x0, 0x430, 0x0, 0x70, 0x0, 0x420, 0x0, 0x60, 0x0, 0x410, 0x0, 0x50, 0x0, 0x400, 0x0, 0x40, 0x0, 0x470, 0x0, 0x30, 0x0, 0x460, 0x0, 0x20, 0x0, 0x450, 0x0, 0x10, 0x0, 0x440, 0x0, 0x0,
    0x0, 0x0, 0x0, 0x0, 0x0, 0x10, 0x0, 0x10, 0x0, 0x0, 0x0, 0x0, 0x0, 0x10, 0x0, 0x10, 0x0, 0x0, 0x0, 0x0, 0x0, 0x10, 0x0, 0x10, 0x0, 0x0, 0x0, 0x0, 0x0, 0x10, 0x0, 0x10,
    0x0, 0x0, 0x0, 0x0, 0x0, 0x10, 0x0, 0x10, 0x0, 0x0, 0x0, 0x0, 0x0, 0x10, 0x0, 0x10, 0x0, 0x0, 0x0, 0x0, 0x0, 0x10, 0x0, 0x10, 0x0, 0x0, 0x0, 0x0, 0x0, 0x10, 0x0, 0x10,
    0x0, 0x10, 0x0, 0x10, 0x0, 0x0, 0x0, 0x0, 0x0, 0x10, 0x0, 0x10, 0x0, 0x0, 0x0, 0x0, 0x0, 0x10, 0x0, 0x10, 0x0, 0x0, 0x0, 0x0, 0x0, 0x10, 0x0, 0x10, 0x0, 0x0, 0x0, 0x0,
    0x0, 0x10, 0x0, 0x10, 0x0, 0x0, 0x0, 0x0, 0x0, 0x10, 0x0, 0x10, 0x0, 0x0, 0x0, 0x0, 0x0, 0x10, 0x0, 0x10, 0x0, 0x0, 0x0, 0x0, 0x0, 0x10, 0x0, 0x10, 0x0, 0x0, 0x0, 0x0,
    0x0, 0x0, 0x0, 0x0, 0x0, 0x10, 0x0, 0x10, 0x0, 0x0, 0x0, 0x0, 0x0, 0x10, 0x0, 0x10, 0x0, 0x0, 0x0, 0x0, 0x0, 0x10, 0x0, 0x10, 0x0, 0x0, 0x0, 0x0, 0x0, 0x10, 0x0, 0x10,
    0x0, 0x0, 0x0, 0x0, 0x0, 0x10, 0x0, 0x10, 0x0, 0x0, 0x0, 0x0, 0x0, 0x10, 0x0, 0x10, 0x0, 0x0, 0x0, 0x0, 0x0, 0x10, 0x0, 0x10, 0x0, 0x0, 0x0, 0x0, 0x0, 0x10, 0x0, 0x10,
    0x0, 0x10, 0x0, 0x10, 0x0, 0x0, 0x0, 0x0, 0x0, 0x10, 0x0, 0x10, 0x0, 0x0, 0x0, 0x0, 0x0, 0x10, 0x0, 0x10, 0x0, 0x0, 0x0, 0x0, 0x0, 0x10, 0x0, 0x10, 0x0, 0x0, 0x0, 0x0,
    0x0, 0x10, 0x0, 0x10, 0x0, 0x0, 0x0, 0x0, 0x0, 0x10, 0x0, 0x10, 0x0, 0x0, 0x0, 0x0, 0x0, 0x10, 0x0, 0x10, 0x0, 0x0, 0x0, 0x0, 0x0, 0x10, 0x0, 0x10, 0x0, 0x0, 0x0, 0x0,
    0x0, 0x0, 0x0, 0x0, 0x0, 0x0, 0x0, 0x20, 0x0, 0x4, 0x0, 0x40, 0x0, 0x4, 0x0, 0x60, 0x0, 0x8, 0x2, 0x80, 0x0, 0x8, 0x2, 0xa0, 0x0, 0xc, 0x2, 0xc0, 0x0, 0xc, 0x2, 0xe0,
    0x2, 0x10, 0x4, 0x110, 0x2, 0x10, 0x4, 0x130, 0x2, 0x14, 0x4, 0x150, 0x2, 0x14, 0x4, 0x170, 0x2, 0x18, 0x6, 0x190, 0x2, 0x18, 0x6, 0x1b0, 0x2, 0x1c, 0x6, 0x1d0, 0x2, 0x1c, 0x6, 0x1f0,
    0x4, 0x20, 0x0, 0x200, 0x4, 0x20, 0x0, 0x220, 0x4, 0x24, 0x0, 0x240, 0x4, 0x24, 0x0, 0x260, 0x4, 0x28, 0x2, 0x80, 0x4, 0x28, 0x2, 0xa0, 0x4, 0x2c, 0x2, 0xc0, 0x4, 0x2c, 0x2, 0xe0,
    0x6, 0x30, 0x4, 0x110, 0x6, 0x30, 0x4, 0x130, 0x6, 0x34, 0x4, 0x150, 0x6, 0x34, 0x4, 0x170, 0x6, 0x38, 0x6, 0x190, 0x6, 0x38, 0x6, 0x1b0, 0x6, 0x3c, 0x6, 0x1d0, 0x6, 0x3c, 0x6, 0x1f0,
    0x8, 0x40, 0x8, 0x10, 0x8, 0x40, 0x8, 0x30, 0x8, 0x44, 0x8, 0x50, 0x8, 0x44, 0x8, 0x70, 0x8, 0x48, 0xa, 0x90, 0x8, 0x48, 0xa, 0xb0, 0x8, 0x4c, 0xa, 0xd0, 0x8, 0x4c, 0xa, 0xf0,
    0xa, 0x50, 0xc, 0x100, 0xa, 0x50, 0xc, 0x120, 0xa, 0x54, 0xc, 0x140, 0xa, 0x54, 0xc, 0x160, 0xa, 0x58, 0xe, 0x180, 0xa, 0x58, 0xe, 0x1a0, 0xa, 0x5c, 0xe, 0x1c0, 0xa, 0x5c, 0xe, 0x1e0,
    0xc, 0x60, 0x8, 0x210, 0xc, 0x60, 0x8, 0x230, 0xc, 0x64, 0x8, 0x250, 0xc, 0x64, 0x8, 0x270, 0xc, 0x68, 0xa, 0x90, 0xc, 0x68, 0xa, 0xb0, 0xc, 0x6c, 0xa, 0xd0, 0xc, 0x6c, 0xa, 0xf0,
    0xe, 0x70, 0xc, 0x100, 0xe, 0x70, 0xc, 0x120, 0xe, 0x74, 0xc, 0x140, 0xe, 0x74, 0xc, 0x160, 0xe, 0x78, 0xe, 0x180, 0xe, 0x78, 0xe, 0x1a0, 0xe, 0x7c, 0xe, 0x1c0, 0xe, 0x7c, 0xe, 0x1e0,
    0x0, 0x20, 0x40, 0x60, 0x220, 0x200, 0x260, 0x240, 0x80, 0xa0, 0xc0, 0xe0, 0x2a0, 0x280, 0x2e0, 0x2c0, 0x440, 0x460, 0x400, 0x420, 0x660, 0x640, 0x620, 0x600, 0x4c0, 0x4e0, 0x480, 0x4a0, 0x6e0, 0x6c0, 0x6a0, 0x680,
    0x110, 0x130, 0x150, 0x170, 0x330, 0x310, 0x370, 0x350, 0x190, 0x1b0, 0x1d0, 0x1f0, 0x3b0, 0x390, 0x3f0, 0x3d0, 0x550, 0x570, 0x510, 0x530, 0x770, 0x750, 0x730, 0x710, 0x5d0, 0x5f0, 0x590, 0x5b0, 0x7f0, 0x7d0, 0x7b0, 0x790,
    0x800, 0x820, 0x840, 0x860, 0xa20, 0xa00, 0xa60, 0xa40, 0x880, 0x8a0, 0x8c0, 0x8e0, 0xaa0, 0xa80, 0xae0, 0xac0, 0xc40, 0xc60, 0xc00, 0xc20, 0xe60, 0xe40, 0xe20, 0xe00, 0xcc0, 0xce0, 0xc80, 0xca0, 0xee0, 0xec0, 0xea0, 0xe80,
    0x910, 0x930, 0x950, 0x970, 0xb30, 0xb10, 0xb70, 0xb50, 0x990, 0x9b0, 0x9d0, 0x9f0, 0xbb0, 0xb90, 0xbf0, 0xbd0, 0xd50, 0xd70, 0xd10, 0xd30, 0xf70, 0xf50, 0xf30, 0xf10, 0xdd0, 0xdf0, 0xd90, 0xdb0, 0xff0, 0xfd0, 0xfb0, 0xf90,
    0x10, 0x30, 0x50, 0x70, 0x230, 0x210, 0x270, 0x250, 0x90, 0xb0, 0xd0, 0xf0, 0x2b0, 0x290, 0x2f0, 0x2d0, 0x450, 0x470, 0x410, 0x430, 0x670, 0x650, 0x630, 0x610, 0x4d0, 0x4f0, 0x490, 0x4b0, 0x6f0, 0x6d0, 0x6b0, 0x690,
    0x100, 0x120, 0x140, 0x160, 0x320, 0x300, 0x360, 0x340, 0x180, 0x1a0, 0x1c0, 0x1e0, 0x3a0, 0x380, 0x3e0, 0x3c0, 0x540, 0x560, 0x500, 0x520, 0x760, 0x740, 0x720, 0x700, 0x5c0, 0x5e0, 0x580, 0x5a0, 0x7e0, 0x7c0, 0x7a0, 0x780,
    0x810, 0x830, 0x850, 0x870, 0xa30, 0xa10, 0xa70, 0xa50, 0x890, 0x8b0, 0x8d0, 0x8f0, 0xab0, 0xa90, 0xaf0, 0xad0, 0xc50, 0xc70, 0xc10, 0xc30, 0xe70, 0xe50, 0xe30, 0xe10, 0xcd0, 0xcf0, 0xc90, 0xcb0, 0xef0, 0xed0, 0xeb0, 0xe90,
    0x900, 0x920, 0x940, 0x960, 0xb20, 0xb00, 0xb60, 0xb40, 0x980, 0x9a0, 0x9c0, 0x9e0, 0xba0, 0xb80, 0xbe0, 0xbc0, 0xd40, 0xd60, 0xd00, 0xd20, 0xf60, 0xf40, 0xf20, 0xf00, 0xdc0, 0xde0, 0xd80, 0xda0, 0xfe0, 0xfc0, 0xfa0, 0xf80,
    0x0, 0x0, 0x0, 0x400, 0x80, 0x8, 0x20, 0x400, 0x20, 0x10, 0x8, 0x400, 0xa0, 0x18, 0x28, 0x400, 0x110, 0x0, 0x40, 0x400, 0x190, 0x8, 0x60, 0x400, 0x130, 0x10, 0x48, 0x400, 0x1b0, 0x18, 0x68, 0x400,
    0x40, 0x0, 0x10, 0x400, 0xc0, 0x8, 0x30, 0x400, 0x60, 0x10, 0x18, 0x400, 0xe0, 0x18, 0x38, 0x400, 0x150, 0x0, 0x50, 0x400, 0x1d0, 0x8, 0x70, 0x400, 0x170, 0x10, 0x58, 0x400, 0x1f0, 0x18, 0x78, 0x400,
    0x200, 0x20, 0x80, 0x0, 0x280, 0x28, 0xa0, 0x0, 0x220, 0x30, 0x88, 0x0, 0x2a0, 0x38, 0xa8, 0x0, 0x310, 0x20, 0xc0, 0x0, 0x390, 0x28, 0xe0, 0x0, 0x330, 0x30, 0xc8, 0x0, 0x3b0, 0x38, 0xe8, 0x0,
    0x240, 0x20, 0x90, 0x0, 0x2c0, 0x28, 0xb0, 0x0, 0x260, 0x30, 0x98, 0x0, 0x2e0, 0x38, 0xb8, 0x0, 0x350, 0x20, 0xd0, 0x0, 0x3d0, 0x28, 0xf0, 0x0, 0x370, 0x30, 0xd8, 0x0, 0x3f0, 0x38, 0xf8, 0x0,
    0x10, 0x0, 0x0, 0x0, 0x90, 0x8, 0x20, 0x0, 0x30, 0x10, 0x8, 0x0, 0xb0, 0x18, 0x28, 0x0, 0x100, 0x0, 0x40, 0x0, 0x180, 0x8, 0x60, 0x0, 0x120, 0x10, 0x48, 0x0, 0x1a0, 0x18, 0x68, 0x0,
    0x50, 0x0, 0x10, 0x0, 0xd0, 0x8, 0x30, 0x0, 0x70, 0x10, 0x18, 0x0, 0xf0, 0x18, 0x38, 0x0, 0x140, 0x0, 0x50, 0x0, 0x1c0, 0x8, 0x70, 0x0, 0x160, 0x10, 0x58, 0x0, 0x1e0, 0x18, 0x78, 0x0,
    0x210, 0x20, 0x80, 0x400, 0x290, 0x28, 0xa0, 0x400, 0x230, 0x30, 0x88, 0x400, 0x2b0, 0x38, 0xa8, 0x400, 0x300, 0x20, 0xc0, 0x400, 0x380, 0x28, 0xe0, 0x400, 0x320, 0x30, 0xc8, 0x400, 0x3a0, 0x38, 0xe8, 0x400,
    0x250, 0x20, 0x90, 0x400, 0x2d0, 0x28, 0xb0, 0x400, 0x270, 0x30, 0x98, 0x400, 0x2f0, 0x38, 0xb8, 0x400, 0x340, 0x20, 0xd0, 0x400, 0x3c0, 0x28, 0xf0, 0x400, 0x360, 0x30, 0xd8, 0x400, 0x3e0, 0x38, 0xf8, 0x400,
    0x3f80, 0x0, 0x0, 0x0, 0x3f800000, 0x0, 0x0, 0x0, 0x0, 0x3f80, 0x0, 0x0, 0x0, 0x3f800000, 0x0, 0x0, 0x0, 0x0, 0x3f80, 0x0, 0x0, 0x0, 0x3f800000, 0x0, 0x0, 0x0, 0x0, 0x3f80, 0x0, 0x0, 0x0, 0x3f800000,
    0x0, 0x0, 0x0, 0x0, 0x0, 0x0, 0x0, 0x0, 0x0, 0x0, 0x0, 0x0, 0x0, 0x0, 0x0, 0x0, 0x0, 0x0, 0x0, 0x0, 0x0, 0x0, 0x0, 0x0, 0x0, 0x0, 0x0, 0x0, 0x0, 0x0, 0x0, 0x0,
    0x0, 0x0, 0x0, 0x0, 0x0, 0x0, 0x0, 0x0, 0x0, 0x0, 0x0, 0x0, 0x0, 0x0, 0x0, 0x0, 0x0, 0x0, 0x0, 0x0, 0x0, 0x0, 0x0, 0x0, 0x0, 0x0, 0x0, 0x0, 0x0, 0x0, 0x0, 0x0,
    0x0, 0x0, 0x0, 0x0, 0x0, 0x0, 0x0, 0x0, 0x0, 0x0, 0x0, 0x0, 0x0, 0x0, 0x0, 0x0, 0x0, 0x0, 0x0, 0x0, 0x0, 0x0, 0x0, 0x0, 0x0, 0x0, 0x0, 0x0, 0x0, 0x0, 0x0, 0x0,
    0x0, 0x0, 0x0, 0x0, 0x0, 0x0, 0x0, 0x0, 0x0, 0x0, 0x0, 0x0, 0x0, 0x0, 0x0, 0x0, 0x0, 0x0, 0x0, 0x0, 0x0, 0x0, 0x0, 0x0, 0x0, 0x0, 0x0, 0x0, 0x0, 0x0, 0x0, 0x0,
    0x3f80, 0x0, 0x0, 0x0, 0x3f800000, 0x0, 0x0, 0x0, 0x0, 0x3f80, 0x0, 0x0, 0x0, 0x3f800000, 0x0, 0x0, 0x0, 0x0, 0x3f80, 0x0, 0x0, 0x0, 0x3f800000, 0x0, 0x0, 0x0, 0x0, 0x3f80, 0x0, 0x0, 0x0, 0x3f800000,
    0x0, 0x0, 0x0, 0x0, 0x0, 0x0, 0x0, 0x0, 0x0, 0x0, 0x0, 0x0, 0x0, 0x0, 0x0, 0x0, 0x0, 0x0, 0x0, 0x0, 0x0, 0x0, 0x0, 0x0, 0x0, 0x0, 0x0, 0x0, 0x0, 0x0, 0x0, 0x0,
    0x0, 0x0, 0x0, 0x0, 0x0, 0x0, 0x0, 0x0, 0x0, 0x0, 0x0, 0x0, 0x0, 0x0, 0x0, 0x0, 0x0, 0x0, 0x0, 0x0, 0x0, 0x0, 0x0, 0x0, 0x0, 0x0, 0x0, 0x0, 0x0, 0x0, 0x0, 0x0,
    0x0, 0x0, 0x0, 0x0, 0x0, 0x0, 0x0, 0x0, 0x0, 0x0, 0x0, 0x0, 0x0, 0x0, 0x0, 0x0, 0x0, 0x0, 0x0, 0x0, 0x0, 0x0, 0x0, 0x0, 0x0, 0x0, 0x0, 0x0, 0x0, 0x0, 0x0, 0x0,
    0x0, 0x0, 0x0, 0x0, 0x0, 0x0, 0x0, 0x0, 0x0, 0x0, 0x0, 0x0, 0x0, 0x0, 0x0, 0x0, 0x0, 0x0, 0x0, 0x0, 0x0, 0x0, 0x0, 0x0, 0x0, 0x0, 0x0, 0x0, 0x0, 0x0, 0x0, 0x0,
    0x3f80, 0x0, 0x0, 0x0, 0x3f800000, 0x0, 0x0, 0x0, 0x0, 0x3f80, 0x0, 0x0, 0x0, 0x3f800000, 0x0, 0x0, 0x0, 0x0, 0x3f80, 0x0, 0x0, 0x0, 0x3f800000, 0x0, 0x0, 0x0, 0x0, 0x3f80, 0x0, 0x0, 0x0, 0x3f800000,
    0x0, 0x0, 0x0, 0x0, 0x0, 0x0, 0x0, 0x0, 0x0, 0x0, 0x0, 0x0, 0x0, 0x0, 0x0, 0x0, 0x0, 0x0, 0x0, 0x0, 0x0, 0x0, 0x0, 0x0, 0x0, 0x0, 0x0, 0x0, 0x0, 0x0, 0x0, 0x0,
    0x0, 0x0, 0x0, 0x0, 0x0, 0x0, 0x0, 0x0, 0x0, 0x0, 0x0, 0x0, 0x0, 0x0, 0x0, 0x0, 0x0, 0x0, 0x0, 0x0, 0x0, 0x0, 0x0, 0x0, 0x0, 0x0, 0x0, 0x0, 0x0, 0x0, 0x0, 0x0,
    0x0, 0x0, 0x0, 0x0, 0x0, 0x0, 0x0, 0x0, 0x0, 0x0, 0x0, 0x0, 0x0, 0x0, 0x0, 0x0, 0x0, 0x0, 0x0, 0x0, 0x0, 0x0, 0x0, 0x0, 0x0, 0x0, 0x0, 0x0, 0x0, 0x0, 0x0, 0x0,
    0x0, 0x0, 0x0, 0x0, 0x0, 0x0, 0x0, 0x0, 0x0, 0x0, 0x0, 0x0, 0x0, 0x0, 0x0, 0x0, 0x0, 0x0, 0x0, 0x0, 0x0, 0x0, 0x0, 0x0, 0x0, 0x0, 0x0, 0x0, 0x0, 0x0, 0x0, 0x0,
    0x3f80, 0x0, 0x0, 0x0, 0x3f800000, 0x0, 0x0, 0x0, 0x0, 0x3f80, 0x0, 0x0, 0x0, 0x3f800000, 0x0, 0x0, 0x0, 0x0, 0x3f80, 0x0, 0x0, 0x0, 0x3f800000, 0x0, 0x0, 0x0, 0x0, 0x3f80, 0x0, 0x0, 0x0, 0x3f800000,
    0x0, 0x0, 0x0, 0x0, 0x110, 0x180, 0x40, 0x8, 0x220, 0x200, 0x88, 0x10, 0x330, 0x380, 0xc8, 0x18, 0x440, 0x400, 0x110, 0x40, 0x550, 0x580, 0x150, 0x48, 0x660, 0x600, 0x198, 0x50, 0x770, 0x780, 0x1d8, 0x58,
    0x880, 0x800, 0x220, 0x88, 0x990, 0x980, 0x260, 0x80, 0xaa0, 0xa00, 0x2a8, 0x98, 0xbb0, 0xb80, 0x2e8, 0x90, 0xcc0, 0xc00, 0x330, 0xc8, 0xdd0, 0xd80, 0x370, 0xc0, 0xee0, 0xe00, 0x3b8, 0xd8, 0xff0, 0xf80, 0x3f8, 0xd0,
    0x1000, 0x1000, 0x400, 0x20, 0x1110, 0x1180, 0x440, 0x28, 0x1220, 0x1200, 0x488, 0x30, 0x1330, 0x1380, 0x4c8, 0x38, 0x440, 0x1400, 0x510, 0x60, 0x550, 0x1580, 0x550, 0x68, 0x660, 0x1600, 0x598, 0x70, 0x770, 0x1780, 0x5d8, 0x78,
    0x880, 0x1800, 0x620, 0xa8, 0x990, 0x1980, 0x660, 0xa0, 0xaa0, 0x1a00, 0x6a8, 0xb8, 0xbb0, 0x1b80, 0x6e8, 0xb0, 0xcc0, 0x1c00, 0x730, 0xe8, 0xdd0, 0x1d80, 0x770, 0xe0, 0xee0, 0x1e00, 0x7b8, 0xf8, 0xff0, 0x1f80, 0x7f8, 0xf0,
    0x10, 0x20, 0x8, 0x220, 0x100, 0x1a0, 0x48, 0x228, 0x230, 0x220, 0x80, 0x230, 0x320, 0x3a0, 0xc0, 0x238, 0x450, 0x420, 0x118, 0x260, 0x540, 0x5a0, 0x158, 0x268, 0x670, 0x620, 0x190, 0x270, 0x760, 0x7a0, 0x1d0, 0x278,
    0x890, 0x820, 0x228, 0x2a8, 0x980, 0x9a0, 0x268, 0x2a0, 0xab0, 0xa20, 0x2a0, 0x2b8, 0xba0, 0xba0, 0x2e0, 0x2b0, 0xcd0, 0xc20, 0x338, 0x2e8, 0xdc0, 0xda0, 0x378, 0x2e0, 0xef0, 0xe20, 0x3b0, 0x2f8, 0xfe0, 0xfa0, 0x3f0, 0x2f0,
    0x1010, 0x1020, 0x408, 0x200, 0x1100, 0x11a0, 0x448, 0x208, 0x1230, 0x1220, 0x480, 0x210, 0x1320, 0x13a0, 0x4c0, 0x218, 0x450, 0x1420, 0x518, 0x240, 0x540, 0x15a0, 0x558, 0x248, 0x670, 0x1620, 0x590, 0x250, 0x760, 0x17a0, 0x5d0, 0x258,
    0x890, 0x1820, 0x628, 0x288, 0x980, 0x19a0, 0x668, 0x280, 0xab0, 0x1a20, 0x6a0, 0x298, 0xba0, 0x1ba0, 0x6e0, 0x290, 0xcd0, 0x1c20, 0x738, 0x2c8, 0xdc0, 0x1da0, 0x778, 0x2c0, 0xef0, 0x1e20, 0x7b0, 0x2d8, 0xfe0, 0x1fa0, 0x7f0, 0x2d0,
    0x0, 0x0, 0x0, 0x0, 0x100, 0x20, 0x110, 0x0, 0x200, 0x8, 0x220, 0x0, 0x300, 0x28, 0x330, 0x0, 0x400, 0x140, 0x440, 0x0, 0x500, 0x160, 0x550, 0x0, 0x600, 0x148, 0x660, 0x0, 0x700, 0x168, 0x770, 0x0,
    0x800, 0x280, 0x800, 0x0, 0x900, 0x2a0, 0x910, 0x0, 0xa00, 0x288, 0xa20, 0x0, 0xb00, 0x2a8, 0xb30, 0x0, 0xc00, 0x3c0, 0xc40, 0x0, 0xd00, 0x3e0, 0xd50, 0x0, 0xe00, 0x3c8, 0xe60, 0x0, 0xf00, 0x3e8, 0xf70, 0x0,
    0x1000, 0x10, 0x1000, 0x0, 0x1100, 0x30, 0x1110, 0x0, 0x1200, 0x18, 0x1220, 0x0, 0x1300, 0x38, 0x1330, 0x0, 0x1400, 0x150, 0x1440, 0x0, 0x1500, 0x170, 0x1550, 0x0, 0x1600, 0x158, 0x1660, 0x0, 0x1700, 0x178, 0x1770, 0x0,
    0x1800, 0x290, 0x1800, 0x0, 0x1900, 0x2b0, 0x1910, 0x0, 0x1a00, 0x298, 0x1a20, 0x0, 0x1b00, 0x2b8, 0x1b30, 0x0, 0x1c00, 0x3d0, 0x1c40, 0x0, 0x1d00, 0x3f0, 0x1d50, 0x0, 0x1e00, 0x3d8, 0x1e60, 0x0, 0x1f00, 0x3f8, 0x1f70, 0x0,
    0x20, 0x800, 0x20, 0x0, 0x120, 0x820, 0x130, 0x0, 0x220, 0x808, 0x200, 0x0, 0x320, 0x828, 0x310, 0x0, 0x420, 0x940, 0x460, 0x0, 0x520, 0x960, 0x570, 0x0, 0x620, 0x948, 0x640, 0x0, 0x720, 0x968, 0x750, 0x0,
    0x820, 0xa80, 0x820, 0x0, 0x920, 0xaa0, 0x930, 0x0, 0xa20, 0xa88, 0xa00, 0x0, 0xb20, 0xaa8, 0xb10, 0x0, 0xc20, 0xbc0, 0xc60, 0x0, 0xd20, 0xbe0, 0xd70, 0x0, 0xe20, 0xbc8, 0xe40, 0x0, 0xf20, 0xbe8, 0xf50, 0x0,
    0x1020, 0x810, 0x1020, 0x0, 0x1120, 0x830, 0x1130, 0x0, 0x1220, 0x818, 0x1200, 0x0, 0x1320, 0x838, 0x1310, 0x0, 0x1420, 0x950, 0x1460, 0x0, 0x1520, 0x970, 0x1570, 0x0, 0x1620, 0x958, 0x1640, 0x0, 0x1720, 0x978, 0x1750, 0x0,
    0x1820, 0xa90, 0x1820, 0x0, 0x1920, 0xab0, 0x1930, 0x0, 0x1a20, 0xa98, 0x1a00, 0x0, 0x1b20, 0xab8, 0x1b10, 0x0, 0x1c20, 0xbd0, 0x1c60, 0x0, 0x1d20, 0xbf0, 0x1d70, 0x0, 0x1e20, 0xbd8, 0x1e40, 0x0, 0x1f20, 0xbf8, 0x1f50, 0x0};

DI u32 bucket_start(u32 b) { u32 s = (b * 1048576u + BM - 1) / BM; return s < (u32)NN ? s : (u32)NN; }
DI u32x4 make_srd(const void* p, u32 stride, u32 nrec) {
  unsigned long long a = (unsigned long long)p;
  u32x4 s; s[0] = (u32)a; s[1] = ((u32)(a >> 32) & 0xffffu) | (stride << 16); s[2] = nrec; s[3] = 0x20000u; return s;
}
template <class T> DI T ld_dev(const T* p) { return __hip_atomic_load(p, __ATOMIC_RELAXED, __HIP_MEMORY_SCOPE_AGENT); }
DI u32x16 cat4(u32x4 a, u32x4 b, u32x4 c, u32x4 d) {
  return (u32x16){a[0], a[1], a[2], a[3], b[0], b[1], b[2], b[3], c[0], c[1], c[2], c[3], d[0], d[1], d[2], d[3]};
}

struct Args { const u16* x; const u16* ea; const int* src; const int* dst; const u16* wmsg; const u16* wupd; char* ws; u16* out; };
using KArgs = const __attribute__((address_space(4))) Args*;

__global__ __launch_bounds__(512, 2) void mpnn_fused(Args) {
  __shared__ __attribute__((aligned(4096))) char lds[163840];
  asm volatile("" :: "v"((u32)(size_t)lds));
  const int tid0 = threadIdx.x;
  const u32 b = blockIdx.x;
  KArgs ka = (KArgs)__builtin_amdgcn_kernarg_segment_ptr();
  f32x16 agg0, agg1;
  u32x16 w3, wua, wub;
  u32x8 ident, epi;
  u32 nlast, more, tag;
  asm volatile(
        "s_load_dwordx16 s[84:99], s[0:1], 0\n\t"
        "v_readfirstlane_b32 s53, v244\n\t"
        "v_mov_b32 v246, 0\n\t"
        "s_mov_b32 s59, s78\n\t"
        "s_lshl_b32 s59, s59, 20\n\t"
        "s_add_u32 s59, s59, 26842\n\t"
        "s_mul_hi_u32 s59, s59, 2621493283\n\t"
        "s_lshr_b32 s59, s59, 14\n\t"
        "s_min_u32 s59, s59, 10000\n\t"
        "s_lshl_b32 s59, s59, 8\n\t"
        "s_lshr_b32 s53, s53, 6\n\t"
        "s_mov_b32 s31, m0\n\t"
        "s_lshr_b32 s54, s53, 2\n\t"
        "s_lshr_b32 s56, s53, 1\n\t"
        "s_and_b32 s62, s53, 1\n\t"
        "s_and_b32 s63, s56, 1\n\t"
        "s_xor_b32 s55, s63, s54\n\t"
        "s_xor_b32 s57, s62, s63\n\t"
        "s_lshl_b32 s76, s62, 7\n\t"
        "s_lshl_b32 s29, s53, 13\n\t"
        "s_lshl_b32 s30, s53, 12\n\t"
        "s_add_u32 s29, s29, 98304\n\t"
        "s_add_u32 s30, s30, 65536\n\t"
        "v_and_b32 v245, 63, v244\n\t"
        "v_lshlrev_b32 v245, 4, v245\n\t"
        "s_add_u32 s74, s78, 1\n\t"
        "s_lshl_b32 s74, s74, 20\n\t"
        "s_add_u32 s74, s74, 26842\n\t"
        "s_mul_hi_u32 s74, s74, 2621493283\n\t"
        "s_lshr_b32 s74, s74, 14\n\t"
        "s_min_u32 s74, s74, 10000\n\t"
        "s_lshl_b32 s74, s74, 8\n\t"
        "s_xor_b32 s42, s78, 8\n\t"
        "s_lshl_b32 s42, s42, 20\n\t"
        "s_add_u32 s42, s42, 26842\n\t"
        "s_mul_hi_u32 s42, s42, 2621493283\n\t"
        "s_lshr_b32 s42, s42, 14\n\t"
        "s_min_u32 s42, s42, 10000\n\t"
        "s_lshl_b32 s42, s42, 8\n\t"
        "s_bitcmp1_b32 s78, 3\n\t"
        "s_cbranch_scc1 120f\n\t"
        "s_lshr_b32 s77, s78, 4\n\t"
        "s_and_b32 s50, s78, 7\n\t"
        "s_lshl_b32 s77, s77, 3\n\t"
        "s_or_b32 s77, s77, s50\n\t"
        "s_movk_i32 s28, 500\n\t"
        "v_lshlrev_b32 v48, 4, v244\n\t"
        "v_lshlrev_b32 v49, 2, v244\n\t"
        "s_mul_i32 s50, s77, 10000\n\t"
        "v_cmp_gt_u32 s[48:49], s28, v244\n\t"
        "v_add_u32 v51, 8000, v49\n\t"
        "s_waitcnt lgkmcnt(0)\n\t"
        "s_add_u32 s4, s90, s50\n\t"
        "s_addc_u32 s5, s91, 0\n\t"
        "s_mov_b64 exec, s[48:49]\n\t"
        "global_load_dwordx4 v[0:3], v48, s[4:5] nt\n\t"
        "global_load_dword v4, v51, s[4:5] nt\n\t"
        "s_add_u32 s6, s88, s50\n\t"
        "s_addc_u32 s7, s89, 0\n\t"
        "global_load_dwordx4 v[8:11], v48, s[6:7] nt\n\t"
        "global_load_dword v12, v51, s[6:7] nt\n\t"
        "s_mov_b64 exec, -1\n\t"
        "s_mov_b64 s[60:61], s[96:97]\n\t"
        "s_mov_b64 s[8:9], s[48:49]\n\t"
        "global_load_dword v247, v246, s[96:97] sc1\n\t"
        "s_mov_b64 s[64:65], s[84:85]\n\t"
        "s_mov_b32 s66, 2560000\n\t"
        "s_mov_b32 s67, 0x20000\n\t"
        "s_mov_b64 s[16:17], s[92:93]\n\t"
        "s_mov_b32 s18, 69632\n\t"
        "s_mov_b32 s19, 0x20000\n\t"
        "s_mov_b64 s[36:37], s[100:101]\n\t"
        "s_mov_b32 s38, 12288\n\t"
        "s_mov_b32 s39, 0x20000\n\t"
        "v_mov_b64 v[52:53], -1\n\t"
        "v_mov_b64 v[54:55], -1\n\t"
        "v_add_u32 v50, 98304, v48\n\t"
        "ds_write_b128 v50, v[52:55]\n\t"
        "ds_write_b128 v50, v[52:55] offset:8192\n\t"
        "ds_write_b128 v50, v[52:55] offset:16384\n\t"
        "ds_write_b128 v50, v[52:55] offset:24576\n\t"
        "v_mov_b32 v52, 0\n\t"
        "ds_write_b32 v49, v52 offset:10240\n\t"
        "v_lshlrev_b32 v53, 22, v244\n\t"
        "v_lshlrev_b32 v54, 20, v244\n\t"
        "v_mov_b32 v55, 1\n\t"
        "v_add_u32 v54, 0x7d000000, v54\n\t"
        "v_lshrrev_b32 v56, 3, v244\n\t"
        "v_and_b32 v57, 7, v244\n\t"
        "v_lshlrev_b32 v58, 14, v56\n\t"
        "v_add_u32 v56, s77, v56\n\t"
        "v_lshl_or_b32 v58, v57, 4, v58\n\t"
        "v_add_u32 v57, 0, v56\n\t"
        "v_and_b32 v57, 0x7f, v57\n\t"
        "v_lshl_or_b32 v64, v57, 7, v58\n\t"
        "v_add_u32 v57, 32, v56\n\t"
        "v_and_b32 v57, 0x7f, v57\n\t"
        "v_lshl_or_b32 v65, v57, 7, v58\n\t"
        "v_add_u32 v57, 64, v56\n\t"
        "v_and_b32 v57, 0x7f, v57\n\t"
        "v_lshl_or_b32 v66, v57, 7, v58\n\t"
        "v_add_u32 v57, 96, v56\n\t"
        "v_and_b32 v57, 0x7f, v57\n\t"
        "v_lshl_or_b32 v67, v57, 7, v58\n\t"
        "v_lshrrev_b32 v76, 4, v245\n\t"
        "v_lshrrev_b32 v77, 4, v76\n\t"
        "v_xor_b32 v78, v76, v77\n\t"
        "v_lshlrev_b32 v77, 2, v77\n\t"
        "v_lshlrev_b32 v78, 4, v78\n\t"
        "v_xor_b32 v77, v77, v76\n\t"
        "v_lshlrev_b32 v77, 4, v77\n\t"
        "s_add_u32 s12, s96, 65536\n\t"
        "s_addc_u32 s13, s97, 0\n\t"
        "s_mov_b32 s14, 4194304\n\t"
        "s_mov_b32 s15, 0x20000\n\t"
        "s_add_u32 s80, s96, 4259840\n\t"
        "s_addc_u32 s81, s97, 0\n\t"
        "s_mov_b32 s82, 2560000\n\t"
        "s_mov_b32 s83, 0x20000\n\t"
        "s_add_u32 s68, s96, 6852608\n\t"
        "s_addc_u32 s69, s97, 0\n\t"
        "s_mov_b32 s70, 32768\n\t"
        "s_mov_b32 s71, 0x20000\n\t"
        "s_add_u32 s20, s96, 4256768\n\t"
        "s_addc_u32 s21, s97, 0\n\t"
        "s_or_b32 s21, s21, 16777216\n\t"
        "s_mov_b32 s22, 10128\n\t"
        "s_mov_b32 s23, 0x20000\n\t"
        "s_mov_b64 s[24:25], s[86:87]\n\t"
        "s_or_b32 s25, s25, 2097152\n\t"
        "s_mov_b32 s27, 0x20000\n\t"
        "s_mov_b64 s[72:73], s[98:99]\n\t"
        "s_mov_b32 s75, 0x20000\n\t"
        "s_add_u32 s40, s96, 6885376\n\t"
        "s_addc_u32 s41, s97, 0\n\t"
        "s_waitcnt lgkmcnt(0)\n\t"
        "s_barrier\n\t"
        "s_mov_b64 exec, s[8:9]\n\t"
        "s_movk_i32 s28, 26843\n\t"
        "s_waitcnt vmcnt(3)\n\t"
        "v_mul_u32_u24 v16, s28, v0\n\t"
        "v_mul_u32_u24 v17, s28, v1\n\t"
        "v_mul_u32_u24 v18, s28, v2\n\t"
        "v_mul_u32_u24 v19, s28, v3\n\t"
        "v_mul_u32_u24 v20, s28, v4\n\t"
        "v_lshrrev_b32 v16, 18, v16\n\t"
        "v_lshrrev_b32 v17, 18, v17\n\t"
        "v_lshrrev_b32 v18, 18, v18\n\t"
        "v_lshrrev_b32 v19, 18, v19\n\t"
        "v_lshrrev_b32 v20, 18, v20\n\t"
        "v_and_b32 v16, 0x3fc, v16\n\t"
        "ds_add_rtn_u32 v24, v16, v55 offset:10240\n\t"
        "v_and_b32 v17, 0x3fc, v17\n\t"
        "ds_add_rtn_u32 v25, v17, v55 offset:10240\n\t"
        "v_and_b32 v18, 0x3fc, v18\n\t"
        "ds_add_rtn_u32 v26, v18, v55 offset:10240\n\t"
        "v_and_b32 v19, 0x3fc, v19\n\t"
        "ds_add_rtn_u32 v27, v19, v55 offset:10240\n\t"
        "v_and_b32 v20, 0x3fc, v20\n\t"
        "ds_add_rtn_u32 v28, v20, v55 offset:10240\n\t"
        "s_mov_b32 s50, 98304\n\t"
        "v_and_b32 v0, 63, v0\n\t"
        "v_lshl_add_u32 v40, v16, 5, s50\n\t"
        "v_and_b32 v1, 63, v1\n\t"
        "v_lshl_add_u32 v41, v17, 5, s50\n\t"
        "v_and_b32 v2, 63, v2\n\t"
        "v_lshl_add_u32 v42, v18, 5, s50\n\t"
        "v_and_b32 v3, 63, v3\n\t"
        "v_lshl_add_u32 v43, v19, 5, s50\n\t"
        "v_and_b32 v4, 63, v4\n\t"
        "v_lshl_add_u32 v44, v20, 5, s50\n\t"
        "v_or_b32 v1, 0x100000, v1\n\t"
        "v_or_b32 v2, 0x200000, v2\n\t"
        "v_or_b32 v3, 0x300000, v3\n\t"
        "s_waitcnt vmcnt(0)\n\t"
        "v_lshlrev_b32 v8, 6, v8\n\t"
        "v_lshlrev_b32 v9, 6, v9\n\t"
        "v_lshlrev_b32 v10, 6, v10\n\t"
        "v_lshlrev_b32 v11, 6, v11\n\t"
        "v_lshlrev_b32 v12, 6, v12\n\t"
        "v_or3_b32 v32, v0, v8, v53\n\t"
        "v_or3_b32 v33, v1, v9, v53\n\t"
        "v_or3_b32 v34, v2, v10, v53\n\t"
        "v_or3_b32 v35, v3, v11, v53\n\t"
        "v_or3_b32 v36, v4, v12, v54\n\t"
        "s_waitcnt lgkmcnt(0)\n\t"
        "v_max3_u32 v56, v24, v25, v26\n\t"
        "v_lshl_add_u32 v40, v24, 2, v40\n\t"
        "v_lshl_add_u32 v41, v25, 2, v41\n\t"
        "v_lshl_add_u32 v42, v26, 2, v42\n\t"
        "v_lshl_add_u32 v43, v27, 2, v43\n\t"
        "v_lshl_add_u32 v44, v28, 2, v44\n\t"
        "v_max3_u32 v56, v56, v27, v28\n\t"
        "v_cmp_lt_u32 vcc, 31, v56\n\t"
        "s_cbranch_vccnz 84f\n\t"
        "ds_write_b32 v40, v32\n\t"
        "ds_write_b32 v41, v33\n\t"
        "ds_write_b32 v42, v34\n\t"
        "ds_write_b32 v43, v35\n\t"
        "ds_write_b32 v44, v36\n\t"
        "85:\n\t"
        "s_mov_b64 exec, -1\n\t"
        "v_readfirstlane_b32 s79, v247\n\t"
        "s_waitcnt lgkmcnt(0)\n\t"
        "s_barrier\n\t"
        "s_add_u32 s79, s79, 1\n\t"
        "s_cmp_ge_u32 s53, 4\n\t"
        "s_cbranch_scc1 86f\n\t"
        "ds_read_b128 v[0:3], v50\n\t"
        "ds_read_b128 v[4:7], v50 offset:4096\n\t"
        "ds_read_b128 v[8:11], v50 offset:8192\n\t"
        "ds_read_b128 v[12:15], v50 offset:12288\n\t"
        "ds_read_b128 v[16:19], v50 offset:16384\n\t"
        "ds_read_b128 v[20:23], v50 offset:20480\n\t"
        "ds_read_b128 v[24:27], v50 offset:24576\n\t"
        "ds_read_b128 v[28:31], v50 offset:28672\n\t"
        "s_mov_b32 s50, 0\n\t"
        "s_waitcnt lgkmcnt(7)\n\t"
        "buffer_store_dwordx4 v[0:3], v64, s[12:15], s50 offen sc1\n\t"
        "s_add_u32 s50, s50, 524288\n\t"
        "s_waitcnt lgkmcnt(6)\n\t"
        "buffer_store_dwordx4 v[4:7], v65, s[12:15], s50 offen sc1\n\t"
        "s_add_u32 s50, s50, 524288\n\t"
        "s_waitcnt lgkmcnt(5)\n\t"
        "buffer_store_dwordx4 v[8:11], v66, s[12:15], s50 offen sc1\n\t"
        "s_add_u32 s50, s50, 524288\n\t"
        "s_waitcnt lgkmcnt(4)\n\t"
        "buffer_store_dwordx4 v[12:15], v67, s[12:15], s50 offen sc1\n\t"
        "s_add_u32 s50, s50, 524288\n\t"
        "s_waitcnt lgkmcnt(3)\n\t"
        "buffer_store_dwordx4 v[16:19], v64, s[12:15], s50 offen sc1\n\t"
        "s_add_u32 s50, s50, 524288\n\t"
        "s_waitcnt lgkmcnt(2)\n\t"
        "buffer_store_dwordx4 v[20:23], v65, s[12:15], s50 offen sc1\n\t"
        "s_add_u32 s50, s50, 524288\n\t"
        "s_waitcnt lgkmcnt(1)\n\t"
        "buffer_store_dwordx4 v[24:27], v66, s[12:15], s50 offen sc1\n\t"
        "s_add_u32 s50, s50, 524288\n\t"
        "s_waitcnt lgkmcnt(0)\n\t"
        "buffer_store_dwordx4 v[28:31], v67, s[12:15], s50 offen sc1\n\t"
        "s_add_u32 s50, s50, 524288\n\t"
        "s_cmp_lg_u32 s53, 0\n\t"
        "s_cbranch_scc1 87f\n\t"
        "v_mov_b32 v57, 0\n\t"
        "ds_read_b32 v58, v57 offset:11264\n\t"
        "s_lshl_b32 s50, s77, 2\n\t"
        "s_add_u32 s50, s50, 36864\n\t"
        "v_mov_b32 v57, s50\n\t"
        "s_waitcnt lgkmcnt(0)\n\t"
        "global_store_dword v57, v58, s[60:61] sc1\n\t"
        "s_branch 87f\n\t"
        "86:\n\t"
        "s_sub_u32 s28, s53, 4\n\t"
        "s_mov_b32 s48, s28\n\t"
        "88:\n\t"
        "s_lshl_b32 s50, s48, 10\n\t"
        "s_add_u32 m0, s50, 55296\n\t"
        "s_cmp_ge_u32 s48, 5\n\t"
        "s_cselect_b32 s51, 5, 0\n\t"
        "s_sub_u32 s51, s48, s51\n\t"
        "s_and_b32 s51, s51, 3\n\t"
        "s_lshl_b32 s51, s51, 6\n\t"
        "v_xor_b32 v79, s51, v78\n\t"
        "s_add_u32 s50, s50, s59\n\t"
        "s_add_u32 s48, s48, 4\n\t"
        "buffer_load_dwordx4 v79, s[64:67], s50 offen nt lds\n\t"
        "s_cmp_lt_u32 s48, 10\n\t"
        "s_cbranch_scc1 88b\n\t"
        "s_mov_b32 s48, s28\n\t"
        "90:\n\t"
        "s_lshl_b32 s50, s48, 10\n\t"
        "s_add_u32 m0, s50, 14336\n\t"
        "s_add_u32 s50, s50, 0\n\t"
        "s_add_u32 s48, s48, 4\n\t"
        "buffer_load_dwordx4 v245, s[36:39], s50 offen lds\n\t"
        "s_cmp_lt_u32 s48, 12\n\t"
        "s_cbranch_scc1 90b\n\t"
        "s_mov_b32 s48, s28\n\t"
        "89:\n\t"
        "s_lshl_b32 s50, s48, 10\n\t"
        "s_add_u32 m0, s50, 131072\n\t"
        "s_add_u32 s50, s50, 32768\n\t"
        "s_add_u32 s48, s48, 4\n\t"
        "buffer_load_dwordx4 v77, s[16:19], s50 offen lds\n\t"
        "s_cmp_lt_u32 s48, 32\n\t"
        "s_cbranch_scc1 89b\n\t"
        "s_mov_b32 s48, s28\n\t"
        "143:\n\t"
        "s_lshl_b32 s50, s48, 10\n\t"
        "s_add_u32 m0, s50, 26624\n\t"
        "s_add_u32 s50, s50, 65536\n\t"
        "s_add_u32 s48, s48, 4\n\t"
        "buffer_load_dwordx4 v77, s[16:19], s50 offen lds\n\t"
        "s_cmp_lt_u32 s48, 4\n\t"
        "s_cbranch_scc1 143b\n\t"
        "87:\n\t"
        "s_cmp_ge_u32 s53, 4\n\t"
        "s_cbranch_scc1 142f\n\t"
        "v_mov_b32 v60, 11268\n\t"
        "v_mov_b32 v61, 1\n\t"
        "s_waitcnt vmcnt(0)\n\t"
        "s_mov_b64 exec, 1\n\t"
        "ds_add_rtn_u32 v60, v60, v61\n\t"
        "s_mov_b64 exec, -1\n\t"
        "s_waitcnt lgkmcnt(0)\n\t"
        "v_readfirstlane_b32 s50, v60\n\t"
        "s_cmp_lg_u32 s50, 3\n\t"
        "s_cbranch_scc1 142f\n\t"
        "s_lshl_b32 s50, s78, 7\n\t"
        "s_add_u32 s50, s50, 4096\n\t"
        "v_mov_b32 v60, s50\n\t"
        "v_mov_b32 v61, s79\n\t"
        "s_mov_b64 exec, 1\n\t"
        "global_store_dword v60, v61, s[60:61] sc1\n\t"
        "s_mov_b64 exec, -1\n\t"
        "142:\n\t"
        "s_waitcnt vmcnt(0)\n\t"
        "s_barrier\n\t"
        "ds_read_b128 v[248:251], v245 offset:24576\n\t"
        "ds_read_b96 v[252:254], v245 offset:25600\n\t"
        "ds_read_b128 v[48:51], v245 offset:14336\n\t"
        "ds_read_b128 v[192:195], v245 offset:15360\n\t"
        "ds_read_b128 v[80:83], v245 offset:16384\n\t"
        "ds_read_b128 v[84:87], v245 offset:17408\n\t"
        "ds_read_b128 v[88:91], v245 offset:18432\n\t"
        "ds_read_b128 v[92:95], v245 offset:19456\n\t"
        "ds_read_b128 v[96:99], v245 offset:20480\n\t"
        "ds_read_b128 v[100:103], v245 offset:21504\n\t"
        "ds_read_b128 v[152:155], v245 offset:22528\n\t"
        "ds_read_b128 v[156:159], v245 offset:23552\n\t"
        "s_mul_i32 s50, s54, 5120\n\t"
        "s_lshl_b32 s51, s63, 6\n\t"
        "s_add_u32 s48, s51, s76\n\t"
        "s_waitcnt lgkmcnt(10)\n\t"
        "v_add_u32 v248, s50, v248\n\t"
        "v_xor_b32 v253, s48, v253\n\t"
        "v_xor_b32 v249, s76, v249\n\t"
        "s_add_u32 s51, s50, s51\n\t"
        "s_lshl_b32 s49, s54, 6\n\t"
        "s_xor_b32 s48, s48, s49\n\t"
        "v_add_u32 v249, s51, v249\n\t"
        "v_xor_b32 v254, s48, v254\n\t"
        "s_add_u32 s50, s50, 98304\n\t"
        "v_add_u32 v246, 65536, v253\n\t"
        "v_add_u32 v253, 131072, v253\n\t"
        "v_add_u32 v254, s50, v254\n\t"
        "v_xor_b32 v57, 32, v248\n\t"
        "v_xor_b32 v58, 64, v248\n\t"
        "v_xor_b32 v59, 96, v248\n\t"
        "v_xor_b32 v60, 128, v248\n\t"
        "v_xor_b32 v61, 160, v248\n\t"
        "v_xor_b32 v62, 192, v248\n\t"
        "v_xor_b32 v63, 224, v248\n\t"
        "s_waitcnt lgkmcnt(0)\n\t"
        "ds_read_b128 v[0:3], v248 offset:55296\n\t"
        "ds_read_b128 v[4:7], v57 offset:55296\n\t"
        "ds_read_b128 v[8:11], v58 offset:55296\n\t"
        "ds_read_b128 v[12:15], v59 offset:55296\n\t"
        "ds_read_b128 v[16:19], v60 offset:55296\n\t"
        "ds_read_b128 v[20:23], v61 offset:55296\n\t"
        "ds_read_b128 v[24:27], v62 offset:55296\n\t"
        "ds_read_b128 v[28:31], v63 offset:55296\n\t"
        "s_branch 121f\n\t"
        "84:\n\t"
        "s_mov_b64 s[48:49], exec\n\t"
        "v_mov_b32 v56, 0\n\t"
        "s_mul_i32 s50, s77, 20000\n\t"
        "v_cmp_gt_u32 vcc, 32, v24\n\t"
        "s_nop 1\n\t"
        "s_and_b64 exec, s[48:49], vcc\n\t"
        "ds_write_b32 v40, v32\n\t"
        "s_andn2_b64 exec, s[48:49], vcc\n\t"
        "s_cbranch_execz 95f\n\t"
        "ds_add_rtn_u32 v57, v56, v55 offset:11264\n\t"
        "v_mov_b32 v58, v32\n\t"
        "v_lshrrev_b32 v59, 2, v16\n\t"
        "s_waitcnt lgkmcnt(0)\n\t"
        "v_lshl_add_u32 v57, v57, 3, s50\n\t"
        "global_store_dwordx2 v57, v[58:59], s[40:41] sc1\n\t"
        "95:\n\t"
        "s_mov_b64 exec, s[48:49]\n\t"
        "s_nop 1\n\t"
        "v_cmp_gt_u32 vcc, 32, v25\n\t"
        "s_nop 1\n\t"
        "s_and_b64 exec, s[48:49], vcc\n\t"
        "ds_write_b32 v41, v33\n\t"
        "s_andn2_b64 exec, s[48:49], vcc\n\t"
        "s_cbranch_execz 96f\n\t"
        "ds_add_rtn_u32 v57, v56, v55 offset:11264\n\t"
        "v_mov_b32 v58, v33\n\t"
        "v_lshrrev_b32 v59, 2, v17\n\t"
        "s_waitcnt lgkmcnt(0)\n\t"
        "v_lshl_add_u32 v57, v57, 3, s50\n\t"
        "global_store_dwordx2 v57, v[58:59], s[40:41] sc1\n\t"
        "96:\n\t"
        "s_mov_b64 exec, s[48:49]\n\t"
        "s_nop 1\n\t"
        "v_cmp_gt_u32 vcc, 32, v26\n\t"
        "s_nop 1\n\t"
        "s_and_b64 exec, s[48:49], vcc\n\t"
        "ds_write_b32 v42, v34\n\t"
        "s_andn2_b64 exec, s[48:49], vcc\n\t"
        "s_cbranch_execz 97f\n\t"
        "ds_add_rtn_u32 v57, v56, v55 offset:11264\n\t"
        "v_mov_b32 v58, v34\n\t"
        "v_lshrrev_b32 v59, 2, v18\n\t"
        "s_waitcnt lgkmcnt(0)\n\t"
        "v_lshl_add_u32 v57, v57, 3, s50\n\t"
        "global_store_dwordx2 v57, v[58:59], s[40:41] sc1\n\t"
        "97:\n\t"
        "s_mov_b64 exec, s[48:49]\n\t"
        "s_nop 1\n\t"
        "v_cmp_gt_u32 vcc, 32, v27\n\t"
        "s_nop 1\n\t"
        "s_and_b64 exec, s[48:49], vcc\n\t"
        "ds_write_b32 v43, v35\n\t"
        "s_andn2_b64 exec, s[48:49], vcc\n\t"
        "s_cbranch_execz 98f\n\t"
        "ds_add_rtn_u32 v57, v56, v55 offset:11264\n\t"
        "v_mov_b32 v58, v35\n\t"
        "v_lshrrev_b32 v59, 2, v19\n\t"
        "s_waitcnt lgkmcnt(0)\n\t"
        "v_lshl_add_u32 v57, v57, 3, s50\n\t"
        "global_store_dwordx2 v57, v[58:59], s[40:41] sc1\n\t"
        "98:\n\t"
        "s_mov_b64 exec, s[48:49]\n\t"
        "s_nop 1\n\t"
        "v_cmp_gt_u32 vcc, 32, v28\n\t"
        "s_nop 1\n\t"
        "s_and_b64 exec, s[48:49], vcc\n\t"
        "ds_write_b32 v44, v36\n\t"
        "s_andn2_b64 exec, s[48:49], vcc\n\t"
        "s_cbranch_execz 99f\n\t"
        "ds_add_rtn_u32 v57, v56, v55 offset:11264\n\t"
        "v_mov_b32 v58, v36\n\t"
        "v_lshrrev_b32 v59, 2, v20\n\t"
        "s_waitcnt lgkmcnt(0)\n\t"
        "v_lshl_add_u32 v57, v57, 3, s50\n\t"
        "global_store_dwordx2 v57, v[58:59], s[40:41] sc1\n\t"
        "99:\n\t"
        "s_mov_b64 exec, s[48:49]\n\t"
        "s_nop 1\n\t"
        "s_branch 85b\n\t"
        "120:\n\t"
        "v_lshrrev_b32 v76, 4, v245\n\t"
        "v_lshrrev_b32 v77, 4, v76\n\t"
        "v_xor_b32 v78, v76, v77\n\t"
        "v_lshlrev_b32 v77, 2, v77\n\t"
        "v_lshlrev_b32 v78, 4, v78\n\t"
        "v_xor_b32 v77, v77, v76\n\t"
        "v_lshlrev_b32 v77, 4, v77\n\t"
        "s_add_u32 s28, s53, 10\n\t"
        "s_waitcnt lgkmcnt(0)\n\t"
        "s_mov_b64 s[64:65], s[84:85]\n\t"
        "s_mov_b32 s66, 2560000\n\t"
        "s_mov_b32 s67, 0x20000\n\t"
        "s_mov_b64 s[16:17], s[92:93]\n\t"
        "s_mov_b32 s18, 69632\n\t"
        "s_mov_b32 s19, 0x20000\n\t"
        "s_mov_b64 s[36:37], s[100:101]\n\t"
        "s_mov_b32 s38, 12288\n\t"
        "s_mov_b32 s39, 0x20000\n\t"
        "s_mov_b32 s48, s53\n\t"
        "100:\n\t"
        "s_lshl_b32 s50, s48, 10\n\t"
        "s_add_u32 m0, s50, 0\n\t"
        "s_cmp_ge_u32 s48, 5\n\t"
        "s_cselect_b32 s51, 5, 0\n\t"
        "s_sub_u32 s51, s48, s51\n\t"
        "s_and_b32 s51, s51, 3\n\t"
        "s_lshl_b32 s51, s51, 6\n\t"
        "v_xor_b32 v79, s51, v78\n\t"
        "s_add_u32 s50, s50, s42\n\t"
        "s_add_u32 s48, s48, 8\n\t"
        "buffer_load_dwordx4 v79, s[64:67], s50 offen nt lds\n\t"
        "s_cmp_lt_u32 s48, 10\n\t"
        "s_cbranch_scc1 100b\n\t"
        "s_mov_b32 s48, s53\n\t"
        "101:\n\t"
        "s_lshl_b32 s50, s48, 10\n\t"
        "s_add_u32 m0, s50, 55296\n\t"
        "s_cmp_ge_u32 s48, 5\n\t"
        "s_cselect_b32 s51, 5, 0\n\t"
        "s_sub_u32 s51, s48, s51\n\t"
        "s_and_b32 s51, s51, 3\n\t"
        "s_lshl_b32 s51, s51, 6\n\t"
        "v_xor_b32 v79, s51, v78\n\t"
        "s_add_u32 s50, s50, s59\n\t"
        "s_add_u32 s48, s48, 8\n\t"
        "buffer_load_dwordx4 v79, s[64:67], s50 offen nt lds\n\t"
        "s_cmp_lt_u32 s48, 10\n\t"
        "s_cbranch_scc1 101b\n\t"
        "s_cmp_ge_u32 s53, 2\n\t"
        "s_cbranch_scc1 105f\n\t"
        "s_mov_b32 s48, s28\n\t"
        "106:\n\t"
        "s_lshl_b32 s50, s48, 10\n\t"
        "s_add_u32 m0, s50, 14336\n\t"
        "s_add_u32 s50, s50, 0\n\t"
        "s_add_u32 s48, s48, 8\n\t"
        "buffer_load_dwordx4 v245, s[36:39], s50 offen lds\n\t"
        "s_cmp_lt_u32 s48, 12\n\t"
        "s_cbranch_scc1 106b\n\t"
        "105:\n\t"
        "s_mov_b32 s48, s53\n\t"
        "102:\n\t"
        "s_lshl_b32 s50, s48, 10\n\t"
        "s_add_u32 m0, s50, 65536\n\t"
        "s_add_u32 s50, s50, 0\n\t"
        "s_add_u32 s48, s48, 8\n\t"
        "buffer_load_dwordx4 v77, s[16:19], s50 offen lds\n\t"
        "s_cmp_lt_u32 s48, 32\n\t"
        "s_cbranch_scc1 102b\n\t"
        "s_mov_b64 s[60:61], s[96:97]\n\t"
        "global_load_dword v247, v246, s[96:97] sc1\n\t"
        "s_lshr_b32 s8, s78, 4\n\t"
        "s_and_b32 s50, s78, 7\n\t"
        "s_lshl_b32 s8, s8, 3\n\t"
        "s_or_b32 s8, s8, s50\n\t"
        "s_add_u32 s12, s96, 65536\n\t"
        "s_addc_u32 s13, s97, 0\n\t"
        "s_mov_b32 s14, 4194304\n\t"
        "s_mov_b32 s15, 0x20000\n\t"
        "s_add_u32 s80, s96, 4259840\n\t"
        "s_addc_u32 s81, s97, 0\n\t"
        "s_mov_b32 s82, 2560000\n\t"
        "s_mov_b32 s83, 0x20000\n\t"
        "s_add_u32 s68, s96, 6852608\n\t"
        "s_addc_u32 s69, s97, 0\n\t"
        "s_mov_b32 s70, 32768\n\t"
        "s_mov_b32 s71, 0x20000\n\t"
        "s_add_u32 s20, s96, 4256768\n\t"
        "s_addc_u32 s21, s97, 0\n\t"
        "s_or_b32 s21, s21, 16777216\n\t"
        "s_mov_b32 s22, 10128\n\t"
        "s_mov_b32 s23, 0x20000\n\t"
        "s_mov_b64 s[24:25], s[86:87]\n\t"
        "s_or_b32 s25, s25, 2097152\n\t"
        "s_mov_b32 s27, 0x20000\n\t"
        "s_mov_b64 s[72:73], s[98:99]\n\t"
        "s_mov_b32 s75, 0x20000\n\t"
        "s_add_u32 s40, s96, 6885376\n\t"
        "s_addc_u32 s41, s97, 0\n\t"
        "s_waitcnt vmcnt(1)\n\t"
        "s_barrier\n\t"
        "s_mov_b32 s48, s53\n\t"
        "103:\n\t"
        "s_lshl_b32 s50, s48, 10\n\t"
        "s_add_u32 m0, s50, 131072\n\t"
        "s_add_u32 s50, s50, 32768\n\t"
        "s_add_u32 s48, s48, 8\n\t"
        "buffer_load_dwordx4 v77, s[16:19], s50 offen lds\n\t"
        "s_cmp_lt_u32 s48, 32\n\t"
        "s_cbranch_scc1 103b\n\t"
        "s_mov_b32 s48, s53\n\t"
        "104:\n\t"
        "s_lshl_b32 s50, s48, 10\n\t"
        "s_add_u32 m0, s50, 14336\n\t"
        "s_add_u32 s50, s50, 0\n\t"
        "s_add_u32 s48, s48, 8\n\t"
        "buffer_load_dwordx4 v245, s[36:39], s50 offen lds\n\t"
        "s_cmp_lt_u32 s48, 10\n\t"
        "s_cbranch_scc1 104b\n\t"
        "s_cmp_ge_u32 s53, 4\n\t"
        "s_cbranch_scc1 144f\n\t"
        "s_mov_b32 s48, s53\n\t"
        "145:\n\t"
        "s_lshl_b32 s50, s48, 10\n\t"
        "s_add_u32 m0, s50, 26624\n\t"
        "s_add_u32 s50, s50, 65536\n\t"
        "s_add_u32 s48, s48, 8\n\t"
        "buffer_load_dwordx4 v77, s[16:19], s50 offen lds\n\t"
        "s_cmp_lt_u32 s48, 4\n\t"
        "s_cbranch_scc1 145b\n\t"
        "144:\n\t"
        "s_mov_b64 s[32:33], s[94:95]\n\t"
        "s_mov_b32 s34, 65536\n\t"
        "s_mov_b32 s35, 0x20000\n\t"
        "s_cmp_gt_u32 s8, 7\n\t"
        "s_cbranch_scc1 80f\n\t"
        "v_lshrrev_b32 v66, 4, v245\n\t"
        "v_and_b32 v67, 31, v66\n\t"
        "v_lshrrev_b32 v68, 5, v66\n\t"
        "v_lshlrev_b32 v69, 2, v67\n\t"
        "v_lshrrev_b32 v70, 1, v67\n\t"
        "v_and_b32 v69, 16, v69\n\t"
        "v_and_b32 v70, 12, v70\n\t"
        "v_and_b32 v67, 3, v67\n\t"
        "v_or3_b32 v67, v67, v69, v70\n\t"
        "s_lshr_b32 s50, s8, 2\n\t"
        "s_lshl_b32 s50, s50, 15\n\t"
        "s_lshl_b32 s51, s53, 12\n\t"
        "s_add_u32 s50, s50, s51\n\t"
        "s_and_b32 s51, s8, 3\n\t"
        "s_lshl_b32 s51, s51, 6\n\t"
        "s_add_u32 s50, s50, s51\n\t"
        "v_lshlrev_b32 v67, 1, v67\n\t"
        "v_lshl_or_b32 v67, v68, 11, v67\n\t"
        "buffer_load_ushort v232, v67, s[32:35], s50 offen\n\t"
        "buffer_load_ushort v233, v67, s[32:35], s50 offen offset:256\n\t"
        "buffer_load_ushort v234, v67, s[32:35], s50 offen offset:512\n\t"
        "buffer_load_ushort v235, v67, s[32:35], s50 offen offset:768\n\t"
        "buffer_load_ushort v236, v67, s[32:35], s50 offen offset:1024\n\t"
        "buffer_load_ushort v237, v67, s[32:35], s50 offen offset:1280\n\t"
        "buffer_load_ushort v238, v67, s[32:35], s50 offen offset:1536\n\t"
        "buffer_load_ushort v239, v67, s[32:35], s50 offen offset:1792\n\t"
        "80:\n\t"
        "ds_read_b128 v[248:251], v245 offset:24576\n\t"
        "ds_read_b96 v[252:254], v245 offset:25600\n\t"
        "s_mul_i32 s50, s54, 5120\n\t"
        "s_lshl_b32 s51, s63, 6\n\t"
        "s_add_u32 s48, s51, s76\n\t"
        "s_waitcnt lgkmcnt(0)\n\t"
        "v_add_u32 v248, s50, v248\n\t"
        "v_xor_b32 v253, s48, v253\n\t"
        "v_xor_b32 v249, s76, v249\n\t"
        "s_add_u32 s51, s50, s51\n\t"
        "s_lshl_b32 s49, s54, 6\n\t"
        "s_xor_b32 s48, s48, s49\n\t"
        "v_add_u32 v249, s51, v249\n\t"
        "v_xor_b32 v254, s48, v254\n\t"
        "s_add_u32 s50, s50, 98304\n\t"
        "v_add_u32 v246, 65536, v253\n\t"
        "v_add_u32 v253, 131072, v253\n\t"
        "v_add_u32 v254, s50, v254\n\t"
        "v_xor_b32 v57, 32, v248\n\t"
        "v_xor_b32 v58, 64, v248\n\t"
        "v_xor_b32 v59, 96, v248\n\t"
        "v_xor_b32 v60, 128, v248\n\t"
        "v_xor_b32 v61, 160, v248\n\t"
        "v_xor_b32 v62, 192, v248\n\t"
        "v_xor_b32 v63, 224, v248\n\t"
        "v_mov_b32 v64, 0\n\t"
        "v_mov_b32 v65, 126976\n\t"
        "ds_write_b32 v65, v64\n\t"
        "ds_read_b128 v[0:3], v248 offset:55296\n\t"
        "ds_read_b128 v[32:35], v248\n\t"
        "ds_read_b64_tr_b16 v[104:105], v246\n\t"
        "ds_read_b64_tr_b16 v[106:107], v246 offset:1024\n\t"
        "ds_read_b128 v[4:7], v57 offset:55296\n\t"
        "ds_read_b128 v[36:39], v57\n\t"
        "ds_read_b64_tr_b16 v[108:109], v246 offset:4096\n\t"
        "ds_read_b64_tr_b16 v[110:111], v246 offset:5120\n\t"
        "ds_read_b128 v[8:11], v58 offset:55296\n\t"
        "ds_read_b128 v[40:43], v58\n\t"
        "ds_read_b64_tr_b16 v[112:113], v246 offset:8192\n\t"
        "ds_read_b64_tr_b16 v[114:115], v246 offset:9216\n\t"
        "s_waitcnt lgkmcnt(8)\n\t"
        "v_mfma_f32_32x32x16_bf16 v[200:215], v[104:107], v[0:3], 0\n\t"
        "ds_read_b128 v[12:15], v59 offset:55296\n\t"
        "ds_read_b128 v[44:47], v59\n\t"
        "ds_read_b64_tr_b16 v[116:117], v246 offset:12288\n\t"
        "ds_read_b64_tr_b16 v[118:119], v246 offset:13312\n\t"
        "v_mfma_f32_32x32x16_bf16 v[216:231], v[104:107], v[32:35], 0\n\t"
        "s_waitcnt lgkmcnt(8)\n\t"
        "v_mfma_f32_32x32x16_bf16 v[200:215], v[108:111], v[4:7], v[200:215]\n\t"
        "ds_read_b128 v[16:19], v60 offset:55296\n\t"
        "ds_read_b128 v[48:51], v60\n\t"
        "ds_read_b64_tr_b16 v[120:121], v246 offset:16384\n\t"
        "ds_read_b64_tr_b16 v[122:123], v246 offset:17408\n\t"
        "v_mfma_f32_32x32x16_bf16 v[216:231], v[108:111], v[36:39], v[216:231]\n\t"
        "s_waitcnt lgkmcnt(8)\n\t"
        "v_mfma_f32_32x32x16_bf16 v[200:215], v[112:115], v[8:11], v[200:215]\n\t"
        "ds_read_b128 v[20:23], v61 offset:55296\n\t"
        "ds_read_b128 v[52:55], v61\n\t"
        "ds_read_b64_tr_b16 v[124:125], v246 offset:20480\n\t"
        "ds_read_b64_tr_b16 v[126:127], v246 offset:21504\n\t"
        "v_mfma_f32_32x32x16_bf16 v[216:231], v[112:115], v[40:43], v[216:231]\n\t"
        "s_waitcnt lgkmcnt(8)\n\t"
        "v_mfma_f32_32x32x16_bf16 v[200:215], v[116:119], v[12:15], v[200:215]\n\t"
        "ds_read_b128 v[24:27], v62 offset:55296\n\t"
        "ds_read_b128 v[136:139], v62\n\t"
        "ds_read_b64_tr_b16 v[128:129], v246 offset:24576\n\t"
        "ds_read_b64_tr_b16 v[130:131], v246 offset:25600\n\t"
        "v_mfma_f32_32x32x16_bf16 v[216:231], v[116:119], v[44:47], v[216:231]\n\t"
        "s_waitcnt lgkmcnt(8)\n\t"
        "v_mfma_f32_32x32x16_bf16 v[200:215], v[120:123], v[16:19], v[200:215]\n\t"
        "ds_read_b128 v[28:31], v63 offset:55296\n\t"
        "ds_read_b128 v[140:143], v63\n\t"
        "ds_read_b64_tr_b16 v[132:133], v246 offset:28672\n\t"
        "ds_read_b64_tr_b16 v[134:135], v246 offset:29696\n\t"
        "v_mfma_f32_32x32x16_bf16 v[216:231], v[120:123], v[48:51], v[216:231]\n\t"
        "s_waitcnt lgkmcnt(8)\n\t"
        "v_mfma_f32_32x32x16_bf16 v[200:215], v[124:127], v[20:23], v[200:215]\n\t"
        "v_mfma_f32_32x32x16_bf16 v[216:231], v[124:127], v[52:55], v[216:231]\n\t"
        "s_waitcnt lgkmcnt(4)\n\t"
        "v_mfma_f32_32x32x16_bf16 v[200:215], v[128:131], v[24:27], v[200:215]\n\t"
        "v_mfma_f32_32x32x16_bf16 v[216:231], v[128:131], v[136:139], v[216:231]\n\t"
        "s_waitcnt lgkmcnt(0)\n\t"
        "v_mfma_f32_32x32x16_bf16 v[200:215], v[132:135], v[28:31], v[200:215]\n\t"
        "v_mfma_f32_32x32x16_bf16 v[216:231], v[132:135], v[140:143], v[216:231]\n\t"
        "s_mov_b32 s48, 0xbfb8aa3b\n\t"
        "s_mov_b32 s49, 0xbfb8aa3b\n\t"
        "v_xor_b32 v64, 16, v254\n\t"
        "s_nop 8\n\t"
        "v_pk_mul_f32 v[200:201], v[200:201], s[48:49]\n\t"
        "v_pk_mul_f32 v[202:203], v[202:203], s[48:49]\n\t"
        "v_pk_mul_f32 v[204:205], v[204:205], s[48:49]\n\t"
        "v_pk_mul_f32 v[206:207], v[206:207], s[48:49]\n\t"
        "v_pk_mul_f32 v[208:209], v[208:209], s[48:49]\n\t"
        "v_pk_mul_f32 v[210:211], v[210:211], s[48:49]\n\t"
        "v_pk_mul_f32 v[212:213], v[212:213], s[48:49]\n\t"
        "v_pk_mul_f32 v[214:215], v[214:215], s[48:49]\n\t"
        "v_cvt_pk_bf16_f32 v200, v200, v201\n\t"
        "v_cvt_pk_bf16_f32 v201, v202, v203\n\t"
        "v_cvt_pk_bf16_f32 v202, v204, v205\n\t"
        "v_cvt_pk_bf16_f32 v203, v206, v207\n\t"
        "v_cvt_pk_bf16_f32 v204, v208, v209\n\t"
        "v_cvt_pk_bf16_f32 v205, v210, v211\n\t"
        "v_cvt_pk_bf16_f32 v206, v212, v213\n\t"
        "v_cvt_pk_bf16_f32 v207, v214, v215\n\t"
        "s_mov_b32 exec_lo, 0xfffff\n\t"
        "s_mov_b32 exec_hi, 0xfffff\n\t"
        "ds_write_b128 v254, v[200:203] offset:10240\n\t"
        "ds_write_b128 v64, v[204:207] offset:10240\n\t"
        "s_mov_b64 exec, -1\n\t"
        "v_pk_mul_f32 v[216:217], v[216:217], s[48:49]\n\t"
        "v_pk_mul_f32 v[218:219], v[218:219], s[48:49]\n\t"
        "v_pk_mul_f32 v[220:221], v[220:221], s[48:49]\n\t"
        "v_pk_mul_f32 v[222:223], v[222:223], s[48:49]\n\t"
        "v_pk_mul_f32 v[224:225], v[224:225], s[48:49]\n\t"
        "v_pk_mul_f32 v[226:227], v[226:227], s[48:49]\n\t"
        "v_pk_mul_f32 v[228:229], v[228:229], s[48:49]\n\t"
        "v_pk_mul_f32 v[230:231], v[230:231], s[48:49]\n\t"
        "v_cvt_pk_bf16_f32 v216, v216, v217\n\t"
        "v_cvt_pk_bf16_f32 v217, v218, v219\n\t"
        "v_cvt_pk_bf16_f32 v218, v220, v221\n\t"
        "v_cvt_pk_bf16_f32 v219, v222, v223\n\t"
        "v_cvt_pk_bf16_f32 v220, v224, v225\n\t"
        "v_cvt_pk_bf16_f32 v221, v226, v227\n\t"
        "v_cvt_pk_bf16_f32 v222, v228, v229\n\t"
        "v_cvt_pk_bf16_f32 v223, v230, v231\n\t"
        "s_mov_b32 exec_lo, 0xfffff\n\t"
        "s_mov_b32 exec_hi, 0xfffff\n\t"
        "ds_write_b128 v254, v[216:219]\n\t"
        "ds_write_b128 v64, v[220:223]\n\t"
        "s_mov_b64 exec, -1\n\t"
        "s_waitcnt vmcnt(0) lgkmcnt(0)\n\t"
        "v_readfirstlane_b32 s79, v247\n\t"
        "s_barrier\n\t"
        "s_add_u32 s79, s79, 1\n\t"
        "s_cmp_lt_u32 s53, 4\n\t"
        "s_cbranch_scc1 112f\n\t"
        "s_sub_u32 s50, s53, 4\n\t"
        "s_lshl_b32 s50, s50, 10\n\t"
        "v_add_u32 v64, s50, v245\n\t"
        "v_add_u32 v65, 98304, v64\n\t"
        "ds_read_b128 v[200:203], v65\n\t"
        "ds_read_b128 v[204:207], v65 offset:4096\n\t"
        "ds_read_b128 v[208:211], v65 offset:8192\n\t"
        "ds_read_b128 v[212:215], v65 offset:12288\n\t"
        "ds_read_b128 v[216:219], v65 offset:16384\n\t"
        "v_lshrrev_b32 v66, 4, v64\n\t"
        "s_sub_u32 s51, s59, 10240\n\t"
        "v_and_b32 v66, 0x70, v66\n\t"
        "s_cmp_lt_u32 s53, 6\n\t"
        "s_cselect_b32 s48, s42, s51\n\t"
        "v_xor_b32 v224, v66, v64\n\t"
        "v_add_u32 v225, 0x1000, v224\n\t"
        "v_add_u32 v226, 0x2000, v224\n\t"
        "v_add_u32 v227, 0x3000, v224\n\t"
        "v_add_u32 v228, 0x4000, v224\n\t"
        "s_waitcnt lgkmcnt(4)\n\t"
        "buffer_store_dwordx4 v[200:203], v224, s[80:83], s42 offen sc1\n\t"
        "s_waitcnt lgkmcnt(3)\n\t"
        "buffer_store_dwordx4 v[204:207], v225, s[80:83], s42 offen sc1\n\t"
        "s_waitcnt lgkmcnt(2)\n\t"
        "buffer_store_dwordx4 v[208:211], v226, s[80:83], s48 offen sc1\n\t"
        "s_waitcnt lgkmcnt(1)\n\t"
        "buffer_store_dwordx4 v[212:215], v227, s[80:83], s51 offen sc1\n\t"
        "s_waitcnt lgkmcnt(0)\n\t"
        "buffer_store_dwordx4 v[216:219], v228, s[80:83], s51 offen sc1\n\t"
        "112:\n\t"
        "s_cmp_gt_u32 s8, 7\n\t"
        "s_cbranch_scc1 91f\n\t"
        "v_lshl_or_b32 v232, v233, 16, v232\n\t"
        "v_lshl_or_b32 v233, v235, 16, v234\n\t"
        "v_lshl_or_b32 v234, v237, 16, v236\n\t"
        "v_lshl_or_b32 v235, v239, 16, v238\n\t"
        "s_cmp_ge_u32 s8, 4\n\t"
        "s_cbranch_scc1 93f\n\t"
        "s_lshl_b32 s50, s8, 13\n\t"
        "s_lshl_b32 s51, s53, 10\n\t"
        "s_add_u32 s50, s50, s51\n\t"
        "buffer_store_dwordx4 v[232:235], v245, s[68:71], s50 offen sc1\n\t"
        "s_branch 91f\n\t"
        "93:\n\t"
        "v_lshrrev_b32 v66, 7, v245\n\t"
        "v_lshrrev_b32 v67, 4, v245\n\t"
        "v_lshrrev_b32 v68, 8, v245\n\t"
        "v_xor_b32 v67, v67, v68\n\t"
        "v_and_b32 v67, 7, v67\n\t"
        "v_lshlrev_b32 v66, 8, v66\n\t"
        "s_lshl_b32 s50, s62, 6\n\t"
        "v_lshl_or_b32 v66, v67, 4, v66\n\t"
        "v_xor_b32 v66, s50, v66\n\t"
        "s_and_b32 s50, s8, 1\n\t"
        "s_lshl_b32 s50, s50, 14\n\t"
        "s_lshl_b32 s51, s53, 11\n\t"
        "s_add_u32 s50, s50, s51\n\t"
        "s_and_b32 s51, s8, 2\n\t"
        "s_lshl_b32 s51, s51, 6\n\t"
        "s_add_u32 s50, s50, s51\n\t"
        "s_add_u32 s50, s50, 2560000\n\t"
        "s_mov_b32 s82, 2592768\n\t"
        "buffer_store_dwordx4 v[232:235], v66, s[80:83], s50 offen sc1\n\t"
        "s_mov_b32 s82, 2560000\n\t"
        "91:\n\t"
        "ds_read_b128 v[48:51], v245 offset:14336\n\t"
        "ds_read_b128 v[192:195], v245 offset:15360\n\t"
        "ds_read_b128 v[80:83], v245 offset:16384\n\t"
        "ds_read_b128 v[84:87], v245 offset:17408\n\t"
        "ds_read_b128 v[88:91], v245 offset:18432\n\t"
        "ds_read_b128 v[92:95], v245 offset:19456\n\t"
        "ds_read_b128 v[96:99], v245 offset:20480\n\t"
        "ds_read_b128 v[100:103], v245 offset:21504\n\t"
        "ds_read_b128 v[152:155], v245 offset:22528\n\t"
        "ds_read_b128 v[156:159], v245 offset:23552\n\t"
        "121:\n\t"
        "s_waitcnt lgkmcnt(0)\n\t"
        "ds_read_b64_tr_b16 v[64:65], v253\n\t"
        "ds_read_b64_tr_b16 v[66:67], v253 offset:1024\n\t"
        "ds_read_b64_tr_b16 v[68:69], v253 offset:4096\n\t"
        "ds_read_b64_tr_b16 v[70:71], v253 offset:5120\n\t"
        "ds_read_b64_tr_b16 v[72:73], v253 offset:8192\n\t"
        "ds_read_b64_tr_b16 v[74:75], v253 offset:9216\n\t"
        "ds_read_b64_tr_b16 v[76:77], v253 offset:12288\n\t"
        "ds_read_b64_tr_b16 v[78:79], v253 offset:13312\n\t"
        "ds_read_b64_tr_b16 v[136:137], v253 offset:16384\n\t"
        "ds_read_b64_tr_b16 v[138:139], v253 offset:17408\n\t"
        "ds_read_b64_tr_b16 v[140:141], v253 offset:20480\n\t"
        "ds_read_b64_tr_b16 v[142:143], v253 offset:21504\n\t"
        "ds_read_b64_tr_b16 v[144:145], v253 offset:24576\n\t"
        "ds_read_b64_tr_b16 v[146:147], v253 offset:25600\n\t"
        "s_waitcnt lgkmcnt(12)\n\t"
        "v_mfma_f32_32x32x16_bf16 v[164:179], v[64:67], v[0:3], 0\n\t"
        "ds_read_b64_tr_b16 v[148:149], v253 offset:28672\n\t"
        "ds_read_b64_tr_b16 v[150:151], v253 offset:29696\n\t"
        "s_lshl_b32 s48, s53, 4\n\t"
        "s_and_b32 s49, s53, 3\n\t"
        "s_sub_u32 s48, s48, s78\n\t"
        "s_mul_i32 s49, s49, 5\n\t"
        "s_lshr_b32 s58, s59, 8\n\t"
        "v_add_u32 v48, s48, v48\n\t"
        "v_and_b32 v48, 0x7f, v48\n\t"
        "s_waitcnt lgkmcnt(12)\n\t"
        "v_mfma_f32_32x32x16_bf16 v[164:179], v[68:71], v[4:7], v[164:179]\n\t"
        "v_mul_u32_u24 v188, 2500, v48\n\t"
        "v_add_u32 v190, s49, v50\n\t"
        "v_mov_b32 v191, v51\n\t"
        "v_add_u32 v195, s58, v195\n\t"
        "s_mul_i32 s50, s54, 20\n\t"
        "s_add_u32 s50, s50, s58\n\t"
        "v_lshlrev_b32 v195, 2, v195\n\t"
        "s_waitcnt lgkmcnt(10)\n\t"
        "v_mfma_f32_32x32x16_bf16 v[164:179], v[72:75], v[8:11], v[164:179]\n\t"
        "v_add_u32 v198, s50, v190\n\t"
        "v_and_b32 v189, 0xfc, v195\n\t"
        "v_lshlrev_b32 v198, 2, v198\n\t"
        "v_and_b32 v198, 0xfc, v198\n\t"
        "s_mul_i32 s50, s54, 30\n\t"
        "s_add_u32 s50, s50, s55\n\t"
        "s_lshl_b32 s51, s50, 6\n\t"
        "s_waitcnt lgkmcnt(8)\n\t"
        "v_mfma_f32_32x32x16_bf16 v[164:179], v[76:79], v[12:15], v[164:179]\n\t"
        "s_add_u32 s51, s51, 30720\n\t"
        "v_add_u32 v92, s51, v92\n\t"
        "s_lshl_b32 s51, s50, 7\n\t"
        "s_add_u32 s51, s51, 34560\n\t"
        "v_add_u32 v93, s51, v93\n\t"
        "s_lshl_b32 s51, s50, 4\n\t"
        "s_add_u32 s51, s51, 10240\n\t"
        "s_waitcnt lgkmcnt(6)\n\t"
        "v_mfma_f32_32x32x16_bf16 v[164:179], v[136:139], v[16:19], v[164:179]\n\t"
        "v_add_u32 v94, s51, v94\n\t"
        "s_lshl_b32 s50, s54, 1\n\t"
        "s_add_u32 s50, s50, s63\n\t"
        "s_mul_i32 s50, s50, 4480\n\t"
        "s_add_u32 s50, s50, 11200\n\t"
        "v_add_u32 v96, s29, v96\n\t"
        "v_add_u32 v97, s29, v97\n\t"
        "s_waitcnt lgkmcnt(4)\n\t"
        "v_mfma_f32_32x32x16_bf16 v[164:179], v[140:143], v[20:23], v[164:179]\n\t"
        "v_add_u32 v98, s29, v98\n\t"
        "v_add_u32 v99, s29, v99\n\t"
        "v_add_u32 v95, s50, v95\n\t"
        "v_add_u32 v100, s30, v100\n\t"
        "v_or_b32 v101, s76, v101\n\t"
        "s_mul_i32 s50, s54, 26\n\t"
        "s_add_u32 s50, s50, s55\n\t"
        "s_waitcnt lgkmcnt(2)\n\t"
        "v_mfma_f32_32x32x16_bf16 v[164:179], v[144:147], v[24:27], v[164:179]\n\t"
        "s_lshl_b32 s50, s50, 8\n\t"
        "s_add_u32 s50, s50, 41984\n\t"
        "v_cmp_ne_u32 vcc, 0, v103\n\t"
        "v_add_u32 v102, s50, v102\n\t"
        "v_mov_b32 v199, 29392\n\t"
        "s_nop 0\n\t"
        "v_cndmask_b32 v102, v199, v102, vcc\n\t"
        "s_waitcnt lgkmcnt(0)\n\t"
        "v_mfma_f32_32x32x16_bf16 v[164:179], v[148:151], v[28:31], v[164:179]\n\t"
        "v_xor_b32 v49, s76, v49\n\t"
        "s_mov_b32 s48, 0xbfb8aa3b\n\t"
        "v_xor_b32 v50, 64, v49\n\t"
        "s_mov_b32 s49, 0xbfb8aa3b\n\t"
        "ds_read_b64_tr_b16 v[32:33], v49 offset:26624\n\t"
        "ds_read_b64_tr_b16 v[34:35], v49 offset:27648\n\t"
        "ds_read_b64_tr_b16 v[36:37], v50 offset:26624\n\t"
        "ds_read_b64_tr_b16 v[38:39], v50 offset:27648\n\t"
        "v_add_u32 v248, 55296, v248\n\t"
        "s_waitcnt lgkmcnt(0)\n\t"
        "s_barrier\n\t"
        "s_nop 1\n\t"
        "v_pk_mul_f32 v[164:165], v[164:165], s[48:49]\n\t"
        "v_pk_mul_f32 v[166:167], v[166:167], s[48:49]\n\t"
        "v_pk_mul_f32 v[168:169], v[168:169], s[48:49]\n\t"
        "v_pk_mul_f32 v[170:171], v[170:171], s[48:49]\n\t"
        "v_pk_mul_f32 v[172:173], v[172:173], s[48:49]\n\t"
        "v_pk_mul_f32 v[174:175], v[174:175], s[48:49]\n\t"
        "v_pk_mul_f32 v[176:177], v[176:177], s[48:49]\n\t"
        "v_pk_mul_f32 v[178:179], v[178:179], s[48:49]\n\t"
        "v_cvt_pk_bf16_f32 v164, v164, v165\n\t"
        "v_cvt_pk_bf16_f32 v165, v166, v167\n\t"
        "v_cvt_pk_bf16_f32 v166, v168, v169\n\t"
        "v_cvt_pk_bf16_f32 v167, v170, v171\n\t"
        "v_cvt_pk_bf16_f32 v168, v172, v173\n\t"
        "v_cvt_pk_bf16_f32 v169, v174, v175\n\t"
        "v_cvt_pk_bf16_f32 v170, v176, v177\n\t"
        "v_cvt_pk_bf16_f32 v171, v178, v179\n\t"
        "s_mov_b32 exec_lo, 0xfffff\n\t"
        "s_mov_b32 exec_hi, 0xfffff\n\t"
        "ds_write_b128 v249, v[164:167] offset:0\n\t"
        "ds_write_b128 v249, v[168:171] offset:16\n\t"
        "s_mov_b64 exec, -1\n\t"
        "v_lshlrev_b32 v0, 16, v32\n\t"
        "v_and_b32 v1, 0xffff0000, v32\n\t"
        "v_lshlrev_b32 v2, 16, v33\n\t"
        "v_and_b32 v3, 0xffff0000, v33\n\t"
        "v_lshlrev_b32 v4, 16, v34\n\t"
        "v_and_b32 v5, 0xffff0000, v34\n\t"
        "v_lshlrev_b32 v6, 16, v35\n\t"
        "v_and_b32 v7, 0xffff0000, v35\n\t"
        "v_lshlrev_b32 v8, 16, v36\n\t"
        "v_and_b32 v9, 0xffff0000, v36\n\t"
        "v_lshlrev_b32 v10, 16, v37\n\t"
        "v_and_b32 v11, 0xffff0000, v37\n\t"
        "v_lshlrev_b32 v12, 16, v38\n\t"
        "v_and_b32 v13, 0xffff0000, v38\n\t"
        "v_lshlrev_b32 v14, 16, v39\n\t"
        "v_and_b32 v15, 0xffff0000, v39\n\t"
        "v_pk_mul_f32 v[0:1], v[0:1], s[48:49]\n\t"
        "v_pk_mul_f32 v[2:3], v[2:3], s[48:49]\n\t"
        "v_pk_mul_f32 v[4:5], v[4:5], s[48:49]\n\t"
        "v_pk_mul_f32 v[6:7], v[6:7], s[48:49]\n\t"
        "v_pk_mul_f32 v[8:9], v[8:9], s[48:49]\n\t"
        "v_pk_mul_f32 v[10:11], v[10:11], s[48:49]\n\t"
        "v_pk_mul_f32 v[12:13], v[12:13], s[48:49]\n\t"
        "v_pk_mul_f32 v[14:15], v[14:15], s[48:49]\n\t"
        "v_cvt_pk_bf16_f32 v136, v0, v1\n\t"
        "v_cvt_pk_bf16_f32 v137, v2, v3\n\t"
        "v_cvt_pk_bf16_f32 v138, v4, v5\n\t"
        "v_cvt_pk_bf16_f32 v139, v6, v7\n\t"
        "v_cvt_pk_bf16_f32 v140, v8, v9\n\t"
        "v_cvt_pk_bf16_f32 v141, v10, v11\n\t"
        "v_cvt_pk_bf16_f32 v142, v12, v13\n\t"
        "v_cvt_pk_bf16_f32 v143, v14, v15\n\t"
        "s_cmp_lg_u32 s53, 0\n\t"
        "s_cbranch_scc1 123f\n\t"
        "v_lshlrev_b32 v74, 4, v245\n\t"
        "v_lshlrev_b32 v75, 3, v245\n\t"
        "v_and_b32 v74, 0x3800, v74\n\t"
        "v_and_b32 v75, 0x380, v75\n\t"
        "v_or_b32 v74, v74, v75\n\t"
        "v_add_u32 v74, 4096, v74\n\t"
        "v_add_u32 v75, 0x4000, v74\n\t"
        "global_load_dword v72, v74, s[60:61] sc1\n\t"
        "global_load_dword v73, v75, s[60:61] sc1\n\t"
        "123:\n\t"
        "v_lshlrev_b32 v0, 4, v244\n\t"
        "v_mov_b64 v[4:5], 0\n\t"
        "v_mov_b64 v[6:7], 0\n\t"
        "v_mov_b64 v[8:9], -1\n\t"
        "v_mov_b64 v[10:11], -1\n\t"
        "v_bfe_u32 v1, v244, 1, 1\n\t"
        "v_bfe_i32 v2, v244, 2, 1\n\t"
        "v_lshlrev_b32 v1, 4, v1\n\t"
        "v_mov_b32 v3, 0x3f80\n\t"
        "ds_write_b128 v0, v[4:7] offset:10240\n\t"
        "v_lshlrev_b32 v1, v1, v3\n\t"
        "ds_write_b128 v0, v[4:7] offset:18432\n\t"
        "v_and_b32 v13, v1, v2\n\t"
        "ds_write_b128 v0, v[8:11] offset:30720\n\t"
        "v_xor_b32 v12, v1, v13\n\t"
        "v_mov_b32 v15, v13\n\t"
        "v_mov_b32 v14, v12\n\t"
        "ds_write_b128 v0, v[12:15] offset:41984\n\t"
        "s_cmp_lt_u32 s53, 5\n\t"
        "s_cselect_b64 exec, -1, 0\n\t"
        "ds_write_b128 v0, v[12:15] offset:50176\n\t"
        "s_cmp_lt_u32 s53, 4\n\t"
        "s_cselect_b64 exec, -1, 0\n\t"
        "ds_write_b128 v0, v[4:7] offset:26624\n\t"
        "s_cmp_lt_u32 s53, 3\n\t"
        "s_cselect_b64 exec, -1, 0\n\t"
        "ds_write_b128 v0, v[8:11] offset:38912\n\t"
        "s_mov_b64 exec, -1\n\t"
        "v_mov_b32 v40, 1\n\t"
        "v_lshlrev_b32 v0, 5, v244\n\t"
        "v_and_b32 v1, 3, v244\n\t"
        "v_and_b32 v0, 0x3f80, v0\n\t"
        "s_lshl_b32 s28, s78, 14\n\t"
        "v_lshl_or_b32 v0, v1, 4, v0\n\t"
        "v_mov_b32 v65, v81\n\t"
        "v_mov_b32 v67, v83\n\t"
        "v_mov_b32 v69, v85\n\t"
        "v_mov_b32 v71, v87\n\t"
        "v_mov_b64 v[104:105], 0\n\t"
        "v_mov_b64 v[106:107], 0\n\t"
        "v_mov_b64 v[108:109], 0\n\t"
        "v_mov_b64 v[110:111], 0\n\t"
        "v_mov_b64 v[112:113], 0\n\t"
        "v_mov_b64 v[114:115], 0\n\t"
        "v_mov_b64 v[116:117], 0\n\t"
        "v_mov_b64 v[118:119], 0\n\t"
        "v_mov_b64 v[120:121], 0\n\t"
        "v_mov_b64 v[122:123], 0\n\t"
        "v_mov_b64 v[124:125], 0\n\t"
        "v_mov_b64 v[126:127], 0\n\t"
        "v_mov_b64 v[128:129], 0\n\t"
        "v_mov_b64 v[130:131], 0\n\t"
        "v_mov_b64 v[132:133], 0\n\t"
        "v_mov_b64 v[134:135], 0\n\t"
        "v_mov_b64 v[162:163], 0\n\t"
        "s_bitcmp1_b32 s78, 3\n\t"
        "s_cbranch_scc0 130f\n\t"
        "s_cmp_gt_u32 s8, 7\n\t"
        "s_cselect_b32 s9, 3, 7\n\t"
        "s_xor_b32 s50, s53, 7\n\t"
        "s_cmp_gt_u32 s50, s9\n\t"
        "s_cbranch_scc1 130f\n\t"
        "v_mov_b32 v76, 126976\n\t"
        "v_mov_b32 v77, 1\n\t"
        "s_waitcnt vmcnt(0)\n\t"
        "s_mov_b64 exec, 1\n\t"
        "ds_add_rtn_u32 v76, v76, v77\n\t"
        "s_mov_b64 exec, -1\n\t"
        "s_waitcnt lgkmcnt(0)\n\t"
        "v_readfirstlane_b32 s50, v76\n\t"
        "s_cmp_lg_u32 s50, s9\n\t"
        "s_cbranch_scc1 130f\n\t"
        "s_lshl_b32 s50, s78, 7\n\t"
        "s_add_u32 s50, s50, 4096\n\t"
        "v_mov_b32 v76, s50\n\t"
        "v_mov_b32 v77, s79\n\t"
        "s_mov_b64 exec, 1\n\t"
        "global_store_dword v76, v77, s[60:61] sc1\n\t"
        "s_mov_b64 exec, -1\n\t"
        "130:\n\t"
        "s_cmp_lg_u32 s53, 0\n\t"
        "s_cbranch_scc1 124f\n\t"
        "s_mov_b32 s50, 65536\n\t"
        "125:\n\t"
        "s_waitcnt vmcnt(0)\n\t"
        "v_cmp_ne_u32 vcc, s79, v72\n\t"
        "v_cmp_ne_u32 s[48:49], s79, v73\n\t"
        "s_sub_u32 s50, s50, 1\n\t"
        "s_cbranch_scc1 126f\n\t"
        "s_or_b64 vcc, vcc, s[48:49]\n\t"
        "s_cbranch_scc0 126f\n\t"
        "global_load_dword v72, v74, s[60:61] sc1\n\t"
        "global_load_dword v73, v75, s[60:61] sc1\n\t"
        "s_branch 125b\n\t"
        "126:\n\t"
        "124:\n\t"
        "s_waitcnt lgkmcnt(0)\n\t"
        "s_barrier\n\t"
        "buffer_load_dwordx4 v[180:183], v0, s[12:15], s28 offen sc1\n\t"
        "buffer_load_dwordx4 v[184:187], v0, s[12:15], s28 offen offset:64 sc1\n\t"
        "v_mov_b32 v73, v89\n\t"
        "v_mov_b32 v75, v89\n\t"
        "v_mov_b64 v[164:165], 0\n\t"
        "v_mov_b64 v[166:167], 0\n\t"
        "v_mov_b32 v168, 1.0\n\t"
        "v_mov_b32 v169, 1.0\n\t"
        "v_mov_b32 v170, 1.0\n\t"
        "v_mov_b32 v171, 1.0\n\t"
        "v_mov_b32 v172, 1.0\n\t"
        "s_waitcnt vmcnt(0)\n\t"
        "v_lshlrev_b32 v0, 2, v180\n\t"
        "v_cmp_ne_u32 s[32:33], -1, v180\n\t"
        "v_lshlrev_b32 v1, 2, v181\n\t"
        "v_cmp_ne_u32 s[34:35], -1, v181\n\t"
        "v_lshlrev_b32 v2, 2, v182\n\t"
        "v_cmp_ne_u32 s[36:37], -1, v182\n\t"
        "v_lshlrev_b32 v3, 2, v183\n\t"
        "v_cmp_ne_u32 s[38:39], -1, v183\n\t"
        "v_and_b32 v0, 0xfc, v0\n\t"
        "v_and_b32 v1, 0xfc, v1\n\t"
        "v_and_b32 v2, 0xfc, v2\n\t"
        "v_and_b32 v3, 0xfc, v3\n\t"
        "s_mov_b64 exec, s[32:33]\n\t"
        "ds_add_rtn_u32 v8, v0, v40 offset:29120\n\t"
        "s_mov_b64 exec, s[34:35]\n\t"
        "ds_add_rtn_u32 v9, v1, v40 offset:29120\n\t"
        "s_mov_b64 exec, s[36:37]\n\t"
        "ds_add_rtn_u32 v10, v2, v40 offset:29120\n\t"
        "s_mov_b64 exec, s[38:39]\n\t"
        "ds_add_rtn_u32 v11, v3, v40 offset:29120\n\t"
        "s_mov_b64 exec, -1\n\t"
        "v_bfe_u32 v16, v180, 6, 14\n\t"
        "v_lshrrev_b32 v24, 20, v180\n\t"
        "v_bfe_u32 v17, v181, 6, 14\n\t"
        "v_lshrrev_b32 v25, 20, v181\n\t"
        "v_bfe_u32 v18, v182, 6, 14\n\t"
        "v_lshrrev_b32 v26, 20, v182\n\t"
        "v_bfe_u32 v19, v183, 6, 14\n\t"
        "v_lshrrev_b32 v27, 20, v183\n\t"
        "v_add_u32 v24, v24, v188\n\t"
        "v_add_u32 v25, v25, v188\n\t"
        "v_add_u32 v26, v26, v188\n\t"
        "v_add_u32 v27, v27, v188\n\t"
        "s_lshl_b32 s50, s58, 2\n\t"
        "s_mov_b32 s26, 320000\n\t"
        "v_subrev_u32 v56, s50, v0\n\t"
        "v_subrev_u32 v57, s50, v1\n\t"
        "v_subrev_u32 v58, s50, v2\n\t"
        "v_subrev_u32 v59, s50, v3\n\t"
        "v_and_b32 v56, 0xfc, v56\n\t"
        "v_and_b32 v57, 0xfc, v57\n\t"
        "v_and_b32 v58, 0xfc, v58\n\t"
        "v_and_b32 v59, 0xfc, v59\n\t"
        "v_cmp_gt_u32 vcc, 32, v56\n\t"
        "s_and_b64 exec, vcc, s[32:33]\n\t"
        "s_cbranch_scc0 150f\n\t"
        "buffer_load_dword v247, v24, s[24:27], 0 idxen\n\t"
        "150:\n\t"
        "s_mov_b64 exec, -1\n\t"
        "v_cmp_gt_u32 vcc, 32, v57\n\t"
        "s_and_b64 exec, vcc, s[34:35]\n\t"
        "s_cbranch_scc0 151f\n\t"
        "buffer_load_dword v247, v25, s[24:27], 0 idxen\n\t"
        "151:\n\t"
        "s_mov_b64 exec, -1\n\t"
        "v_cmp_gt_u32 vcc, 32, v58\n\t"
        "s_and_b64 exec, vcc, s[36:37]\n\t"
        "s_cbranch_scc0 152f\n\t"
        "buffer_load_dword v247, v26, s[24:27], 0 idxen\n\t"
        "152:\n\t"
        "s_mov_b64 exec, -1\n\t"
        "v_cmp_gt_u32 vcc, 32, v59\n\t"
        "s_and_b64 exec, vcc, s[38:39]\n\t"
        "s_cbranch_scc0 153f\n\t"
        "buffer_load_dword v247, v27, s[24:27], 0 idxen\n\t"
        "153:\n\t"
        "s_mov_b64 exec, -1\n\t"
        "s_mov_b32 s50, 0x88888888\n\t"
        "s_mov_b32 s51, 0x88888888\n\t"
        "s_and_b64 s[40:41], s[38:39], s[50:51]\n\t"
        "s_cbranch_scc0 10f\n\t"
        "s_lshr_b64 s[50:51], s[40:41], 1\n\t"
        "s_or_b64 s[50:51], s[50:51], s[40:41]\n\t"
        "s_lshr_b64 s[42:43], s[50:51], 2\n\t"
        "s_or_b64 s[50:51], s[50:51], s[42:43]\n\t"
        "v_lshlrev_b32 v4, 2, v184\n\t"
        "v_cmp_ne_u32 s[40:41], -1, v184\n\t"
        "v_lshlrev_b32 v5, 2, v185\n\t"
        "v_cmp_ne_u32 s[42:43], -1, v185\n\t"
        "v_lshlrev_b32 v6, 2, v186\n\t"
        "v_cmp_ne_u32 s[44:45], -1, v186\n\t"
        "v_lshlrev_b32 v7, 2, v187\n\t"
        "v_cmp_ne_u32 s[46:47], -1, v187\n\t"
        "v_and_b32 v4, 0xfc, v4\n\t"
        "v_and_b32 v5, 0xfc, v5\n\t"
        "v_and_b32 v6, 0xfc, v6\n\t"
        "v_and_b32 v7, 0xfc, v7\n\t"
        "s_and_b64 s[40:41], s[40:41], s[50:51]\n\t"
        "s_and_b64 s[42:43], s[42:43], s[50:51]\n\t"
        "s_and_b64 s[44:45], s[44:45], s[50:51]\n\t"
        "s_and_b64 s[46:47], s[46:47], s[50:51]\n\t"
        "s_mov_b64 exec, s[40:41]\n\t"
        "ds_add_rtn_u32 v12, v4, v40 offset:29120\n\t"
        "s_mov_b64 exec, s[42:43]\n\t"
        "ds_add_rtn_u32 v13, v5, v40 offset:29120\n\t"
        "s_mov_b64 exec, s[44:45]\n\t"
        "ds_add_rtn_u32 v14, v6, v40 offset:29120\n\t"
        "s_mov_b64 exec, s[46:47]\n\t"
        "ds_add_rtn_u32 v15, v7, v40 offset:29120\n\t"
        "s_mov_b64 exec, -1\n\t"
        "v_bfe_u32 v20, v184, 6, 14\n\t"
        "v_lshrrev_b32 v28, 20, v184\n\t"
        "v_bfe_u32 v21, v185, 6, 14\n\t"
        "v_lshrrev_b32 v29, 20, v185\n\t"
        "v_bfe_u32 v22, v186, 6, 14\n\t"
        "v_lshrrev_b32 v30, 20, v186\n\t"
        "v_bfe_u32 v23, v187, 6, 14\n\t"
        "v_lshrrev_b32 v31, 20, v187\n\t"
        "v_add_u32 v28, v28, v188\n\t"
        "v_add_u32 v29, v29, v188\n\t"
        "v_add_u32 v30, v30, v188\n\t"
        "v_add_u32 v31, v31, v188\n\t"
        "s_mov_b32 s50, 0x88888888\n\t"
        "s_mov_b32 s51, 0x88888888\n\t"
        "s_and_b64 exec, s[46:47], s[50:51]\n\t"
        "s_cbranch_scc0 11f\n\t"
        "v_mov_b32 v41, 0\n\t"
        "ds_write_b32 v41, v40 offset:29376\n\t"
        "11:\n\t"
        "s_mov_b64 exec, -1\n\t"
        "10:\n\t"
        "s_waitcnt lgkmcnt(0)\n\t"
        "s_barrier\n\t"
        "ds_read_b32 v41, v189 offset:29120\n\t"
        "ds_read_b32 v196, v198 offset:29120\n\t"
        "s_cmp_lg_u32 s53, 0\n\t"
        "s_cbranch_scc1 131f\n\t"
        "v_lshlrev_b32 v78, 4, v245\n\t"
        "v_lshlrev_b32 v79, 3, v245\n\t"
        "v_and_b32 v78, 0x3800, v78\n\t"
        "v_and_b32 v79, 0x380, v79\n\t"
        "v_or_b32 v78, v78, v79\n\t"
        "v_add_u32 v78, 5120, v78\n\t"
        "v_add_u32 v79, 0x4000, v78\n\t"
        "global_load_dword v76, v78, s[60:61] sc1\n\t"
        "global_load_dword v77, v79, s[60:61] sc1\n\t"
        "131:\n\t"
        "s_waitcnt lgkmcnt(1)\n\t"
        "v_add_u32 v42, 3, v41\n\t"
        "v_lshrrev_b32 v42, 2, v42\n\t"
        "s_nop 1\n\t"
        "v_add_u32_dpp v43, v42, v42 row_shr:1 row_mask:0xf bank_mask:0xf bound_ctrl:1\n\t"
        "s_nop 1\n\t"
        "v_add_u32_dpp v43, v43, v43 row_shr:2 row_mask:0xf bank_mask:0xf bound_ctrl:1\n\t"
        "s_nop 1\n\t"
        "v_add_u32_dpp v43, v43, v43 row_shr:4 row_mask:0xf bank_mask:0xf bound_ctrl:1\n\t"
        "s_nop 1\n\t"
        "v_add_u32_dpp v43, v43, v43 row_shr:8 row_mask:0xf bank_mask:0xf bound_ctrl:1\n\t"
        "s_nop 1\n\t"
        "v_add_u32_dpp v43, v43, v43 row_bcast:15 row_mask:0xa bank_mask:0xf\n\t"
        "v_sub_u32 v44, v43, v42\n\t"
        "v_readlane_b32 s48, v43, 31\n\t"
        "v_readlane_b32 s49, v43, 63\n\t"
        "v_lshl_add_u32 v44, v44, 2, v192\n\t"
        "ds_write_b32 v189, v44 offset:29400\n\t"
        "ds_read_b32 v32, v0 offset:29400\n\t"
        "ds_read_b32 v33, v1 offset:29400\n\t"
        "ds_read_b32 v34, v2 offset:29400\n\t"
        "ds_read_b32 v35, v3 offset:29400\n\t"
        "ds_read_b32 v197, v198 offset:29400\n\t"
        "s_max_u32 s50, s48, s49\n\t"
        "s_mov_b32 s28, 0\n\t"
        "s_cmp_le_u32 s50, 208\n\t"
        "s_cselect_b32 s52, 0, 1\n\t"
        "s_cbranch_scc0 20f\n\t"
        "s_waitcnt lgkmcnt(4)\n\t"
        "v_add_u32 v32, v32, v8\n\t"
        "v_lshlrev_b32 v8, 1, v32\n\t"
        "v_lshlrev_b32 v32, 2, v32\n\t"
        "s_waitcnt lgkmcnt(3)\n\t"
        "v_add_u32 v33, v33, v9\n\t"
        "v_lshlrev_b32 v9, 1, v33\n\t"
        "v_lshlrev_b32 v33, 2, v33\n\t"
        "s_waitcnt lgkmcnt(2)\n\t"
        "v_add_u32 v34, v34, v10\n\t"
        "v_lshlrev_b32 v10, 1, v34\n\t"
        "v_lshlrev_b32 v34, 2, v34\n\t"
        "s_waitcnt lgkmcnt(1)\n\t"
        "v_add_u32 v35, v35, v11\n\t"
        "v_lshlrev_b32 v11, 1, v35\n\t"
        "v_lshlrev_b32 v35, 2, v35\n\t"
        "s_mov_b64 exec, s[32:33]\n\t"
        "ds_write_b16 v8, v16 offset:30720\n\t"
        "ds_write_b32 v32, v24 offset:34560\n\t"
        "s_mov_b64 exec, s[34:35]\n\t"
        "ds_write_b16 v9, v17 offset:30720\n\t"
        "ds_write_b32 v33, v25 offset:34560\n\t"
        "s_mov_b64 exec, s[36:37]\n\t"
        "ds_write_b16 v10, v18 offset:30720\n\t"
        "ds_write_b32 v34, v26 offset:34560\n\t"
        "s_mov_b64 exec, s[38:39]\n\t"
        "ds_write_b16 v11, v19 offset:30720\n\t"
        "ds_write_b32 v35, v27 offset:34560\n\t"
        "s_mov_b64 exec, -1\n\t"
        "s_cmp_eq_u64 s[40:41], 0\n\t"
        "s_cbranch_scc1 12f\n\t"
        "ds_read_b32 v36, v4 offset:29400\n\t"
        "ds_read_b32 v37, v5 offset:29400\n\t"
        "ds_read_b32 v38, v6 offset:29400\n\t"
        "ds_read_b32 v39, v7 offset:29400\n\t"
        "s_waitcnt lgkmcnt(3)\n\t"
        "v_add_u32 v36, v36, v12\n\t"
        "v_lshlrev_b32 v12, 1, v36\n\t"
        "v_lshlrev_b32 v36, 2, v36\n\t"
        "s_waitcnt lgkmcnt(2)\n\t"
        "v_add_u32 v37, v37, v13\n\t"
        "v_lshlrev_b32 v13, 1, v37\n\t"
        "v_lshlrev_b32 v37, 2, v37\n\t"
        "s_waitcnt lgkmcnt(1)\n\t"
        "v_add_u32 v38, v38, v14\n\t"
        "v_lshlrev_b32 v14, 1, v38\n\t"
        "v_lshlrev_b32 v38, 2, v38\n\t"
        "s_waitcnt lgkmcnt(0)\n\t"
        "v_add_u32 v39, v39, v15\n\t"
        "v_lshlrev_b32 v15, 1, v39\n\t"
        "v_lshlrev_b32 v39, 2, v39\n\t"
        "s_mov_b64 exec, s[40:41]\n\t"
        "ds_write_b16 v12, v20 offset:30720\n\t"
        "ds_write_b32 v36, v28 offset:34560\n\t"
        "s_mov_b64 exec, s[42:43]\n\t"
        "ds_write_b16 v13, v21 offset:30720\n\t"
        "ds_write_b32 v37, v29 offset:34560\n\t"
        "s_mov_b64 exec, s[44:45]\n\t"
        "ds_write_b16 v14, v22 offset:30720\n\t"
        "ds_write_b32 v38, v30 offset:34560\n\t"
        "s_mov_b64 exec, s[46:47]\n\t"
        "ds_write_b16 v15, v23 offset:30720\n\t"
        "ds_write_b32 v39, v31 offset:34560\n\t"
        "s_mov_b64 exec, -1\n\t"
        "12:\n\t"
        "s_cmp_ge_u32 s53, 4\n\t"
        "s_cbranch_scc1 16f\n\t"
        "s_cmp_eq_u32 s56, 0\n\t"
        "s_cselect_b32 s50, s48, s49\n\t"
        "s_add_u32 s50, s50, 15\n\t"
        "s_lshr_b32 s50, s50, 3\n\t"
        "s_sub_u32 s50, s50, s57\n\t"
        "s_lshr_b32 s50, s50, 1\n\t"
        "s_mul_i32 s51, s56, 30\n\t"
        "s_lshl_b32 s50, s50, 1\n\t"
        "s_add_u32 s51, s51, s57\n\t"
        "s_add_u32 s50, s50, s51\n\t"
        "s_lshl_b32 s50, s50, 6\n\t"
        "s_lshl_b32 s51, s62, 6\n\t"
        "v_add_u32 v51, s50, v193\n\t"
        "v_add_u32 v52, s51, v194\n\t"
        "ds_write_b16 v51, v52 offset:30720\n\t"
        "16:\n\t"
        "s_cmp_eq_u32 s54, 0\n\t"
        "s_cselect_b32 s28, s48, s49\n\t"
        "s_add_u32 s28, s28, 15\n\t"
        "s_lshr_b32 s28, s28, 3\n\t"
        "s_sub_u32 s28, s28, s55\n\t"
        "s_lshr_b32 s28, s28, 1\n\t"
        "20:\n\t"
        "s_cmp_lg_u32 s53, 0\n\t"
        "s_cbranch_scc1 132f\n\t"
        "s_mov_b32 s50, 65536\n\t"
        "133:\n\t"
        "s_waitcnt vmcnt(0)\n\t"
        "v_cmp_ne_u32 vcc, s79, v76\n\t"
        "v_cmp_ne_u32 s[48:49], s79, v77\n\t"
        "s_sub_u32 s50, s50, 1\n\t"
        "s_cbranch_scc1 134f\n\t"
        "s_or_b64 vcc, vcc, s[48:49]\n\t"
        "s_cbranch_scc0 134f\n\t"
        "global_load_dword v76, v78, s[60:61] sc1\n\t"
        "global_load_dword v77, v79, s[60:61] sc1\n\t"
        "s_branch 133b\n\t"
        "134:\n\t"
        "132:\n\t"
        "s_waitcnt lgkmcnt(0)\n\t"
        "s_barrier\n\t"
        "s_cmp_ge_u32 s53, 4\n\t"
        "s_cbranch_scc1 70f\n\t"
        "ds_read_u16 v80, v92\n\t"
        "ds_read_u16 v82, v92 offset:16\n\t"
        "ds_read_u16 v84, v92 offset:32\n\t"
        "ds_read_u16 v86, v92 offset:48\n\t"
        "ds_read_b32 v88, v93\n\t"
        "s_cmp_gt_u32 s28, 0\n\t"
        "s_cselect_b32 s26, 320000, 0\n\t"
        "s_mov_b32 m0, s30\n\t"
        "s_waitcnt lgkmcnt(0)\n\t"
        "buffer_load_dwordx4 v[88:89], s[24:27], 0 idxen offen lds\n\t"
        "s_mov_b32 m0, s29\n\t"
        "s_nop 0\n\t"
        "buffer_load_dwordx4 v[80:81], s[20:23], s76 idxen offen sc1 lds\n\t"
        "buffer_load_dwordx4 v[82:83], s[20:23], s76 idxen offen offset:1024 sc1 lds\n\t"
        "buffer_load_dwordx4 v[84:85], s[20:23], s76 idxen offen offset:2048 sc1 lds\n\t"
        "buffer_load_dwordx4 v[86:87], s[20:23], s76 idxen offen offset:3072 sc1 lds\n\t"
        "70:\n\t"
        "v_mov_b32 v77, 0\n\t"
        "ds_read_b32 v76, v77 offset:29376\n\t"
        "ds_read_b32 v72, v93 offset:256\n\t"
        "ds_read_u16 v64, v92 offset:128\n\t"
        "ds_read_u16 v66, v92 offset:144\n\t"
        "ds_read_u16 v68, v92 offset:160\n\t"
        "ds_read_u16 v70, v92 offset:176\n\t"
        "ds_read_b32 v74, v93 offset:512\n\t"
        "s_cmp_lg_u32 s52, 0\n\t"
        "s_cbranch_scc1 14f\n\t"
        "s_mul_i32 s51, s54, 1024\n\t"
        "v_and_b32 v203, 3, v196\n\t"
        "v_and_b32 v204, -4, v196\n\t"
        "v_add_u32 v203, -1, v203\n\t"
        "v_add3_u32 v204, v204, v197, v191\n\t"
        "v_cmp_lt_u32 vcc, v203, v191\n\t"
        "v_cmp_gt_u32 s[32:33], 4, v191\n\t"
        "v_lshlrev_b32 v204, 3, v204\n\t"
        "v_mov_b64 v[200:201], 0\n\t"
        "v_subrev_u32 v204, s51, v204\n\t"
        "s_and_b64 exec, vcc, s[32:33]\n\t"
        "ds_write_b64 v204, v[200:201] offset:41984\n\t"
        "s_mov_b64 exec, -1\n\t"
        "s_mul_i32 s50, s54, 5120\n\t"
        "v_and_b32 v203, 1, v190\n\t"
        "v_lshl_add_u32 v208, v190, 8, s50\n\t"
        "s_mul_i32 s50, s54, 8960\n\t"
        "s_add_u32 s50, s50, 11200\n\t"
        "v_lshl_or_b32 v208, v203, 7, v208\n\t"
        "v_lshl_add_u32 v209, v190, 5, s50\n\t"
        "v_bfe_u32 v210, v190, 3, 1\n\t"
        "s_lshl_b32 s50, s54, 2\n\t"
        "v_add_u32 v205, 3, v196\n\t"
        "v_or_b32 v210, s50, v210\n\t"
        "s_mul_i32 s50, s54, 240\n\t"
        "v_lshrrev_b32 v201, 2, v197\n\t"
        "v_lshrrev_b32 v200, 2, v205\n\t"
        "v_mov_b32 v202, v191\n\t"
        "v_mov_b32 v207, 0x3f80\n\t"
        "v_cmp_lt_u32 vcc, v202, v200\n\t"
        "s_and_b64 exec, exec, vcc\n\t"
        "s_cbranch_scc0 14f\n\t"
        "13:\n\t"
        "v_add_u32 v203, v201, v202\n\t"
        "v_add_u32 v202, 12, v202\n\t"
        "v_lshlrev_b32 v204, 1, v203\n\t"
        "v_subrev_u32 v203, s50, v203\n\t"
        "ds_write_b16 v204, v208 offset:10240\n\t"
        "v_lshrrev_b32 v205, 1, v203\n\t"
        "v_and_b32 v204, 2, v204\n\t"
        "v_and_or_b32 v204, v203, 4, v204\n\t"
        "v_and_or_b32 v204, v205, 8, v204\n\t"
        "v_xor_b32 v205, v205, v210\n\t"
        "v_lshrrev_b32 v203, 5, v203\n\t"
        "v_bfe_u32 v206, v205, 2, 1\n\t"
        "v_and_b32 v205, 1, v205\n\t"
        "v_mad_u32_u24 v203, v206, 7, v203\n\t"
        "v_lshl_or_b32 v204, v205, 4, v204\n\t"
        "v_mul_u32_u24 v203, 0x280, v203\n\t"
        "v_cmp_lt_u32 vcc, v202, v200\n\t"
        "v_add3_u32 v203, v203, v204, v209\n\t"
        "ds_write_b16 v203, v207\n\t"
        "s_and_b64 exec, exec, vcc\n\t"
        "s_cbranch_scc1 13b\n\t"
        "14:\n\t"
        "s_mov_b64 exec, -1\n\t"
        "v_mov_b64 v[48:49], 0\n\t"
        "v_mov_b64 v[50:51], 0\n\t"
        "v_mov_b64 v[52:53], 0\n\t"
        "v_mov_b64 v[54:55], 0\n\t"
        "v_mov_b64 v[56:57], 0\n\t"
        "v_mov_b64 v[58:59], 0\n\t"
        "s_waitcnt lgkmcnt(0)\n\t"
        "s_barrier\n\t"
        "s_cmp_lt_u32 s53, 4\n\t"
        "s_cbranch_scc1 71f\n\t"
        "s_sleep 127\n\t"
        "s_setprio 1\n\t"
        "ds_read_u16 v80, v92\n\t"
        "ds_read_u16 v82, v92 offset:16\n\t"
        "ds_read_u16 v84, v92 offset:32\n\t"
        "ds_read_u16 v86, v92 offset:48\n\t"
        "ds_read_b32 v88, v93\n\t"
        "s_cmp_gt_u32 s28, 0\n\t"
        "s_cselect_b32 s26, 320000, 0\n\t"
        "s_mov_b32 m0, s30\n\t"
        "s_waitcnt lgkmcnt(0)\n\t"
        "buffer_load_dwordx4 v[88:89], s[24:27], 0 idxen offen lds\n\t"
        "s_mov_b32 m0, s29\n\t"
        "s_nop 0\n\t"
        "buffer_load_dwordx4 v[80:81], s[20:23], s76 idxen offen sc1 lds\n\t"
        "buffer_load_dwordx4 v[82:83], s[20:23], s76 idxen offen offset:1024 sc1 lds\n\t"
        "buffer_load_dwordx4 v[84:85], s[20:23], s76 idxen offen offset:2048 sc1 lds\n\t"
        "buffer_load_dwordx4 v[86:87], s[20:23], s76 idxen offen offset:3072 sc1 lds\n\t"
        "71:\n\t"
        "ds_read_u16 v90, v94\n\t"
        "ds_read_u16 v5, v94 offset:32\n\t"
        "ds_read_b64 v[160:161], v102\n\t"
        "ds_read_u16 v80, v92 offset:256\n\t"
        "ds_read_u16 v82, v92 offset:272\n\t"
        "ds_read_u16 v84, v92 offset:288\n\t"
        "ds_read_u16 v86, v92 offset:304\n\t"
        "ds_read_b32 v88, v93 offset:768\n\t"
        "v_readfirstlane_b32 s50, v76\n\t"
        "s_mov_b32 s77, 1\n\t"
        "s_lshl_b32 s50, s50, 1\n\t"
        "s_or_b32 s52, s52, s50\n\t"
        "s_cmp_gt_u32 s28, 1\n\t"
        "s_cselect_b32 s26, 320000, 0\n\t"
        "s_add_u32 m0, s30, 1024\n\t"
        "s_nop 0\n\t"
        "buffer_load_dwordx4 v[72:73], s[24:27], 0 idxen offen lds\n\t"
        "s_add_u32 m0, s29, 4096\n\t"
        "s_nop 0\n\t"
        "buffer_load_dwordx4 v[64:65], s[20:23], s76 idxen offen sc1 lds\n\t"
        "buffer_load_dwordx4 v[66:67], s[20:23], s76 idxen offen offset:1024 sc1 lds\n\t"
        "buffer_load_dwordx4 v[68:69], s[20:23], s76 idxen offen offset:2048 sc1 lds\n\t"
        "buffer_load_dwordx4 v[70:71], s[20:23], s76 idxen offen offset:3072 sc1 lds\n\t"
        "v_mov_b64 v[78:79], v[74:75]\n\t"
        "v_mov_b64 v[66:67], 0\n\t"
        "v_mov_b64 v[70:71], 0\n\t"
        "s_mov_b32 s58, s28\n\t"
        "s_cmp_eq_u32 s28, 0\n\t"
        "s_cbranch_scc1 3f\n\t"
        "s_cmp_gt_u32 s28, 2\n\t"
        "s_cselect_b32 s26, 320000, 0\n\t"
        "s_add_u32 m0, s30, 2048\n\t"
        "s_waitcnt lgkmcnt(0)\n\t"
        "v_xor_b32 v91, v90, v101\n\t"
        "v_mov_b32 v90, v5\n\t"
        "v_mov_b64 v[32:33], v[78:79]\n\t"
        "ds_read_b64_tr_b16 v[64:65], v91\n\t"
        "ds_read_b64_tr_b16 v[68:69], v91 offset:64\n\t"
        "s_waitcnt vmcnt(5)\n\t"
        "ds_read_b128 v[60:63], v100\n\t"
        "ds_read_b128 v[72:75], v96\n\t"
        "ds_read_b128 v[76:79], v97\n\t"
        "s_waitcnt lgkmcnt(0)\n\t"
        "v_mfma_f32_32x32x16_bf16 v[0:15], v[72:75], v[152:155], 0\n\t"
        "v_mfma_f32_32x32x16_bf16 v[0:15], v[76:79], v[156:159], v[0:15]\n\t"
        "ds_read_b128 v[72:75], v98\n\t"
        "ds_read_b128 v[76:79], v99\n\t"
        "v_mfma_f32_32x32x16_bf16 v[0:15], v[160:163], v[64:67], v[0:15]\n\t"
        "buffer_load_dwordx4 v[32:33], s[24:27], 0 idxen offen lds\n\t"
        "v_mfma_f32_32x32x16_bf16 v[0:15], v[60:63], v[136:139], v[0:15]\n\t"
        "s_cmp_gt_u32 s28, 3\n\t"
        "s_cselect_b32 s26, 320000, 0\n\t"
        "s_mov_b32 m0, s29\n\t"
        "s_nop 0\n\t"
        "s_nop 11\n\t"
        "1:\n\t"
        "s_waitcnt lgkmcnt(0)\n\t"
        "v_xor_b32 v91, v90, v101\n\t"
        "v_mfma_f32_32x32x16_bf16 v[16:31], v[72:75], v[152:155], 0\n\t"
        "buffer_load_dwordx4 v[80:81], s[20:23], s76 idxen offen sc1 lds\n\t"
        "v_rcp_f32 v168, v168\n\t"
        "v_rcp_f32 v169, v169\n\t"
        "v_rcp_f32 v170, v170\n\t"
        "v_mfma_f32_32x32x16_bf16 v[16:31], v[76:79], v[156:159], v[16:31]\n\t"
        "buffer_load_dwordx4 v[82:83], s[20:23], s76 idxen offen offset:1024 sc1 lds\n\t"
        "v_rcp_f32 v171, v171\n\t"
        "v_exp_f32 v32, v0\n\t"
        "v_exp_f32 v33, v1\n\t"
        "v_mfma_f32_32x32x16_bf16 v[16:31], v[160:163], v[68:71], v[16:31]\n\t"
        "buffer_load_dwordx4 v[84:85], s[20:23], s76 idxen offen offset:2048 sc1 lds\n\t"
        "ds_read_b64 v[160:161], v102 offset:512\n\t"
        "v_exp_f32 v34, v2\n\t"
        "v_exp_f32 v35, v3\n\t"
        "v_exp_f32 v36, v4\n\t"
        "v_mfma_f32_32x32x16_bf16 v[16:31], v[60:63], v[140:143], v[16:31]\n\t"
        "buffer_load_dwordx4 v[86:87], s[20:23], s76 idxen offen offset:3072 sc1 lds\n\t"
        "s_xor_b32 s30, s30, 2048\n\t"
        "s_add_u32 m0, s30, 1024\n\t"
        "v_exp_f32 v37, v5\n\t"
        "v_exp_f32 v38, v6\n\t"
        "v_exp_f32 v39, v7\n\t"
        "s_nop 0\n\t"
        "buffer_load_dwordx4 v[88:89], s[24:27], 0 idxen offen lds\n\t"
        "s_waitcnt vmcnt(6)\n\t"
        "ds_read_b128 v[60:63], v100 offset:1024\n\t"
        "ds_read_b128 v[72:75], v96 offset:4096\n\t"
        "ds_read_b128 v[76:79], v97 offset:4096\n\t"
        "ds_read_b64_tr_b16 v[64:65], v91\n\t"
        "s_sub_u32 s77, s77, 1\n\t"
        "s_cbranch_scc1 50f\n\t"
        "51:\n\t"
        "v_exp_f32 v40, v8\n\t"
        "v_exp_f32 v41, v9\n\t"
        "v_exp_f32 v42, v10\n\t"
        "v_mfma_f32_32x32x16_bf16 v[104:119], v[56:59], v[48:51], v[104:119]\n\t"
        "v_exp_f32 v43, v11\n\t"
        "v_exp_f32 v44, v12\n\t"
        "v_exp_f32 v45, v13\n\t"
        "v_exp_f32 v46, v14\n\t"
        "v_exp_f32 v47, v15\n\t"
        "v_pk_mul_f32 v[164:165], v[164:165], v[168:169]\n\t"
        "v_pk_mul_f32 v[166:167], v[166:167], v[170:171]\n\t"
        "v_pk_add_f32 v[32:33], v[32:33], 1.0 op_sel_hi:[1,0]\n\t"
        "v_pk_add_f32 v[36:37], v[36:37], 1.0 op_sel_hi:[1,0]\n\t"
        "v_pk_add_f32 v[40:41], v[40:41], 1.0 op_sel_hi:[1,0]\n\t"
        "v_pk_add_f32 v[44:45], v[44:45], 1.0 op_sel_hi:[1,0]\n\t"
        "v_pk_fma_f32 v[0:1], v[0:1], v[34:35], v[0:1]\n\t"
        "v_pk_fma_f32 v[4:5], v[4:5], v[38:39], v[4:5]\n\t"
        "v_pk_fma_f32 v[8:9], v[8:9], v[42:43], v[8:9]\n\t"
        "v_pk_fma_f32 v[12:13], v[12:13], v[46:47], v[12:13]\n\t"
        "v_cvt_pk_bf16_f32 v54, v164, v165\n\t"
        "v_cvt_pk_bf16_f32 v55, v166, v167\n\t"
        "v_pk_fma_f32 v[0:1], v[2:3], v[32:33], v[0:1]\n\t"
        "v_pk_fma_f32 v[4:5], v[6:7], v[36:37], v[4:5]\n\t"
        "v_pk_fma_f32 v[8:9], v[10:11], v[40:41], v[8:9]\n\t"
        "v_pk_fma_f32 v[12:13], v[14:15], v[44:45], v[12:13]\n\t"
        "v_pk_fma_f32 v[32:33], v[32:33], v[34:35], v[32:33]\n\t"
        "v_pk_fma_f32 v[36:37], v[36:37], v[38:39], v[36:37]\n\t"
        "v_pk_fma_f32 v[40:41], v[40:41], v[42:43], v[40:41]\n\t"
        "v_pk_fma_f32 v[44:45], v[44:45], v[46:47], v[44:45]\n\t"
        "v_xor_b32 v100, 0x800, v100\n\t"
        "v_pk_mul_f32 v[164:165], v[0:1], v[36:37]\n\t"
        "v_pk_mul_f32 v[166:167], v[8:9], v[44:45]\n\t"
        "v_pk_mul_f32 v[168:169], v[32:33], v[36:37]\n\t"
        "v_pk_mul_f32 v[170:171], v[40:41], v[44:45]\n\t"
        "v_pk_fma_f32 v[164:165], v[4:5], v[32:33], v[164:165]\n\t"
        "v_pk_fma_f32 v[166:167], v[12:13], v[40:41], v[166:167]\n\t"
        "v_max3_f32 v172, v172, v168, v169\n\t"
        "v_max3_f32 v172, v172, v170, v171\n\t"
        "s_waitcnt lgkmcnt(0)\n\t"
        "v_mfma_f32_32x32x16_bf16 v[0:15], v[72:75], v[152:155], 0\n\t"
        "ds_read_u16 v80, v92 offset:384\n\t"
        "ds_read_u16 v82, v92 offset:400\n\t"
        "v_rcp_f32 v168, v168\n\t"
        "v_rcp_f32 v169, v169\n\t"
        "v_rcp_f32 v170, v170\n\t"
        "v_mfma_f32_32x32x16_bf16 v[0:15], v[76:79], v[156:159], v[0:15]\n\t"
        "ds_read_b128 v[72:75], v98 offset:4096\n\t"
        "ds_read_b128 v[76:79], v99 offset:4096\n\t"
        "v_rcp_f32 v171, v171\n\t"
        "v_exp_f32 v32, v16\n\t"
        "v_exp_f32 v33, v17\n\t"
        "v_mfma_f32_32x32x16_bf16 v[0:15], v[160:163], v[64:67], v[0:15]\n\t"
        "ds_read_b64_tr_b16 v[68:69], v91 offset:64\n\t"
        "ds_read_u16 v84, v92 offset:416\n\t"
        "v_exp_f32 v34, v18\n\t"
        "v_exp_f32 v35, v19\n\t"
        "v_exp_f32 v36, v20\n\t"
        "v_mfma_f32_32x32x16_bf16 v[0:15], v[60:63], v[136:139], v[0:15]\n\t"
        "ds_read_u16 v86, v92 offset:432\n\t"
        "ds_read_b32 v88, v93 offset:1024\n\t"
        "v_exp_f32 v37, v21\n\t"
        "v_exp_f32 v38, v22\n\t"
        "v_exp_f32 v39, v23\n\t"
        "s_nop 0\n\t"
        "ds_read_u16 v90, v94 offset:64\n\t"
        "v_exp_f32 v40, v24\n\t"
        "v_exp_f32 v41, v25\n\t"
        "v_exp_f32 v42, v26\n\t"
        "v_mfma_f32_32x32x16_bf16 v[120:135], v[56:59], v[52:55], v[120:135]\n\t"
        "v_exp_f32 v43, v27\n\t"
        "v_exp_f32 v44, v28\n\t"
        "v_exp_f32 v45, v29\n\t"
        "v_exp_f32 v46, v30\n\t"
        "v_exp_f32 v47, v31\n\t"
        "v_pk_mul_f32 v[164:165], v[164:165], v[168:169]\n\t"
        "s_sub_u32 s28, s28, 1\n\t"
        "v_pk_mul_f32 v[166:167], v[166:167], v[170:171]\n\t"
        "s_cmp_gt_u32 s28, 3\n\t"
        "v_pk_add_f32 v[32:33], v[32:33], 1.0 op_sel_hi:[1,0]\n\t"
        "s_cselect_b32 s26, 320000, 0\n\t"
        "v_pk_add_f32 v[36:37], v[36:37], 1.0 op_sel_hi:[1,0]\n\t"
        "s_add_u32 m0, s29, 4096\n\t"
        "v_pk_add_f32 v[40:41], v[40:41], 1.0 op_sel_hi:[1,0]\n\t"
        "v_pk_add_f32 v[44:45], v[44:45], 1.0 op_sel_hi:[1,0]\n\t"
        "v_pk_fma_f32 v[16:17], v[16:17], v[34:35], v[16:17]\n\t"
        "v_pk_fma_f32 v[20:21], v[20:21], v[38:39], v[20:21]\n\t"
        "v_pk_fma_f32 v[24:25], v[24:25], v[42:43], v[24:25]\n\t"
        "v_pk_fma_f32 v[28:29], v[28:29], v[46:47], v[28:29]\n\t"
        "v_cvt_pk_bf16_f32 v48, v164, v165\n\t"
        "v_cvt_pk_bf16_f32 v49, v166, v167\n\t"
        "v_pk_fma_f32 v[16:17], v[18:19], v[32:33], v[16:17]\n\t"
        "v_pk_fma_f32 v[20:21], v[22:23], v[36:37], v[20:21]\n\t"
        "v_pk_fma_f32 v[24:25], v[26:27], v[40:41], v[24:25]\n\t"
        "v_pk_fma_f32 v[28:29], v[30:31], v[44:45], v[28:29]\n\t"
        "v_pk_fma_f32 v[32:33], v[32:33], v[34:35], v[32:33]\n\t"
        "v_pk_fma_f32 v[36:37], v[36:37], v[38:39], v[36:37]\n\t"
        "v_pk_fma_f32 v[40:41], v[40:41], v[42:43], v[40:41]\n\t"
        "v_pk_fma_f32 v[44:45], v[44:45], v[46:47], v[44:45]\n\t"
        "v_pk_mul_f32 v[164:165], v[16:17], v[36:37]\n\t"
        "v_pk_mul_f32 v[166:167], v[24:25], v[44:45]\n\t"
        "v_pk_mul_f32 v[168:169], v[32:33], v[36:37]\n\t"
        "v_pk_mul_f32 v[170:171], v[40:41], v[44:45]\n\t"
        "v_pk_fma_f32 v[164:165], v[20:21], v[32:33], v[164:165]\n\t"
        "v_pk_fma_f32 v[166:167], v[28:29], v[40:41], v[166:167]\n\t"
        "v_max3_f32 v172, v172, v168, v169\n\t"
        "s_cmp_eq_u32 s28, 0\n\t"
        "v_max3_f32 v172, v172, v170, v171\n\t"
        "s_cbranch_scc1 2f\n\t"
        "s_waitcnt lgkmcnt(0)\n\t"
        "v_xor_b32 v91, v90, v101\n\t"
        "v_mfma_f32_32x32x16_bf16 v[16:31], v[72:75], v[152:155], 0\n\t"
        "buffer_load_dwordx4 v[80:81], s[20:23], s76 idxen offen sc1 lds\n\t"
        "v_rcp_f32 v168, v168\n\t"
        "v_rcp_f32 v169, v169\n\t"
        "v_rcp_f32 v170, v170\n\t"
        "v_mfma_f32_32x32x16_bf16 v[16:31], v[76:79], v[156:159], v[16:31]\n\t"
        "buffer_load_dwordx4 v[82:83], s[20:23], s76 idxen offen offset:1024 sc1 lds\n\t"
        "v_rcp_f32 v171, v171\n\t"
        "v_exp_f32 v32, v0\n\t"
        "v_exp_f32 v33, v1\n\t"
        "v_mfma_f32_32x32x16_bf16 v[16:31], v[160:163], v[68:71], v[16:31]\n\t"
        "buffer_load_dwordx4 v[84:85], s[20:23], s76 idxen offen offset:2048 sc1 lds\n\t"
        "ds_read_b64 v[160:161], v102 offset:1024\n\t"
        "v_exp_f32 v34, v2\n\t"
        "v_exp_f32 v35, v3\n\t"
        "v_exp_f32 v36, v4\n\t"
        "v_mfma_f32_32x32x16_bf16 v[16:31], v[60:63], v[140:143], v[16:31]\n\t"
        "buffer_load_dwordx4 v[86:87], s[20:23], s76 idxen offen offset:3072 sc1 lds\n\t"
        "ds_read_b128 v[56:59], v95\n\t"
        "s_xor_b32 m0, s30, 2048\n\t"
        "v_exp_f32 v37, v5\n\t"
        "v_exp_f32 v38, v6\n\t"
        "v_exp_f32 v39, v7\n\t"
        "s_nop 0\n\t"
        "buffer_load_dwordx4 v[88:89], s[24:27], 0 idxen offen lds\n\t"
        "s_waitcnt vmcnt(6)\n\t"
        "ds_read_b128 v[60:63], v100\n\t"
        "ds_read_b128 v[72:75], v96\n\t"
        "ds_read_b128 v[76:79], v97\n\t"
        "ds_read_b64_tr_b16 v[64:65], v91\n\t"
        "v_exp_f32 v40, v8\n\t"
        "v_exp_f32 v41, v9\n\t"
        "v_exp_f32 v42, v10\n\t"
        "v_exp_f32 v43, v11\n\t"
        "v_exp_f32 v44, v12\n\t"
        "v_exp_f32 v45, v13\n\t"
        "v_exp_f32 v46, v14\n\t"
        "v_exp_f32 v47, v15\n\t"
        "v_pk_mul_f32 v[164:165], v[164:165], v[168:169]\n\t"
        "v_pk_mul_f32 v[166:167], v[166:167], v[170:171]\n\t"
        "v_pk_add_f32 v[32:33], v[32:33], 1.0 op_sel_hi:[1,0]\n\t"
        "v_pk_add_f32 v[36:37], v[36:37], 1.0 op_sel_hi:[1,0]\n\t"
        "v_pk_add_f32 v[40:41], v[40:41], 1.0 op_sel_hi:[1,0]\n\t"
        "v_pk_add_f32 v[44:45], v[44:45], 1.0 op_sel_hi:[1,0]\n\t"
        "v_pk_fma_f32 v[0:1], v[0:1], v[34:35], v[0:1]\n\t"
        "v_pk_fma_f32 v[4:5], v[4:5], v[38:39], v[4:5]\n\t"
        "v_pk_fma_f32 v[8:9], v[8:9], v[42:43], v[8:9]\n\t"
        "v_pk_fma_f32 v[12:13], v[12:13], v[46:47], v[12:13]\n\t"
        "v_cvt_pk_bf16_f32 v52, v164, v165\n\t"
        "v_cvt_pk_bf16_f32 v53, v166, v167\n\t"
        "v_pk_fma_f32 v[0:1], v[2:3], v[32:33], v[0:1]\n\t"
        "v_pk_fma_f32 v[4:5], v[6:7], v[36:37], v[4:5]\n\t"
        "v_pk_fma_f32 v[8:9], v[10:11], v[40:41], v[8:9]\n\t"
        "v_pk_fma_f32 v[12:13], v[14:15], v[44:45], v[12:13]\n\t"
        "v_pk_fma_f32 v[32:33], v[32:33], v[34:35], v[32:33]\n\t"
        "v_pk_fma_f32 v[36:37], v[36:37], v[38:39], v[36:37]\n\t"
        "v_pk_fma_f32 v[40:41], v[40:41], v[42:43], v[40:41]\n\t"
        "v_pk_fma_f32 v[44:45], v[44:45], v[46:47], v[44:45]\n\t"
        "v_add_u32 v102, v103, v102\n\t"
        "v_pk_mul_f32 v[164:165], v[0:1], v[36:37]\n\t"
        "v_pk_mul_f32 v[166:167], v[8:9], v[44:45]\n\t"
        "v_pk_mul_f32 v[168:169], v[32:33], v[36:37]\n\t"
        "v_pk_mul_f32 v[170:171], v[40:41], v[44:45]\n\t"
        "v_pk_fma_f32 v[164:165], v[4:5], v[32:33], v[164:165]\n\t"
        "v_pk_fma_f32 v[166:167], v[12:13], v[40:41], v[166:167]\n\t"
        "v_max3_f32 v172, v172, v168, v169\n\t"
        "v_max3_f32 v172, v172, v170, v171\n\t"
        "s_waitcnt lgkmcnt(0)\n\t"
        "v_mfma_f32_32x32x16_bf16 v[0:15], v[72:75], v[152:155], 0\n\t"
        "ds_read_u16 v80, v92 offset:512\n\t"
        "ds_read_u16 v82, v92 offset:528\n\t"
        "v_rcp_f32 v168, v168\n\t"
        "v_rcp_f32 v169, v169\n\t"
        "v_rcp_f32 v170, v170\n\t"
        "v_mfma_f32_32x32x16_bf16 v[0:15], v[76:79], v[156:159], v[0:15]\n\t"
        "ds_read_b128 v[72:75], v98\n\t"
        "ds_read_b128 v[76:79], v99\n\t"
        "v_rcp_f32 v171, v171\n\t"
        "v_exp_f32 v32, v16\n\t"
        "v_exp_f32 v33, v17\n\t"
        "v_mfma_f32_32x32x16_bf16 v[0:15], v[160:163], v[64:67], v[0:15]\n\t"
        "ds_read_b64_tr_b16 v[68:69], v91 offset:64\n\t"
        "ds_read_u16 v84, v92 offset:544\n\t"
        "v_exp_f32 v34, v18\n\t"
        "v_exp_f32 v35, v19\n\t"
        "v_exp_f32 v36, v20\n\t"
        "v_mfma_f32_32x32x16_bf16 v[0:15], v[60:63], v[136:139], v[0:15]\n\t"
        "ds_read_u16 v86, v92 offset:560\n\t"
        "ds_read_b32 v88, v93 offset:1280\n\t"
        "v_exp_f32 v37, v21\n\t"
        "v_exp_f32 v38, v22\n\t"
        "v_exp_f32 v39, v23\n\t"
        "s_nop 0\n\t"
        "ds_read_u16 v90, v94 offset:96\n\t"
        "v_exp_f32 v40, v24\n\t"
        "v_exp_f32 v41, v25\n\t"
        "v_exp_f32 v42, v26\n\t"
        "v_exp_f32 v43, v27\n\t"
        "v_exp_f32 v44, v28\n\t"
        "v_exp_f32 v45, v29\n\t"
        "v_exp_f32 v46, v30\n\t"
        "v_exp_f32 v47, v31\n\t"
        "v_pk_mul_f32 v[164:165], v[164:165], v[168:169]\n\t"
        "s_sub_u32 s28, s28, 1\n\t"
        "v_pk_mul_f32 v[166:167], v[166:167], v[170:171]\n\t"
        "s_cmp_gt_u32 s28, 3\n\t"
        "v_pk_add_f32 v[32:33], v[32:33], 1.0 op_sel_hi:[1,0]\n\t"
        "s_cselect_b32 s26, 320000, 0\n\t"
        "v_pk_add_f32 v[36:37], v[36:37], 1.0 op_sel_hi:[1,0]\n\t"
        "s_add_u32 m0, s29, 0\n\t"
        "v_pk_add_f32 v[40:41], v[40:41], 1.0 op_sel_hi:[1,0]\n\t"
        "v_pk_add_f32 v[44:45], v[44:45], 1.0 op_sel_hi:[1,0]\n\t"
        "v_pk_fma_f32 v[16:17], v[16:17], v[34:35], v[16:17]\n\t"
        "v_pk_fma_f32 v[20:21], v[20:21], v[38:39], v[20:21]\n\t"
        "v_pk_fma_f32 v[24:25], v[24:25], v[42:43], v[24:25]\n\t"
        "v_pk_fma_f32 v[28:29], v[28:29], v[46:47], v[28:29]\n\t"
        "v_cvt_pk_bf16_f32 v50, v164, v165\n\t"
        "v_cvt_pk_bf16_f32 v51, v166, v167\n\t"
        "v_pk_fma_f32 v[16:17], v[18:19], v[32:33], v[16:17]\n\t"
        "v_pk_fma_f32 v[20:21], v[22:23], v[36:37], v[20:21]\n\t"
        "v_pk_fma_f32 v[24:25], v[26:27], v[40:41], v[24:25]\n\t"
        "v_pk_fma_f32 v[28:29], v[30:31], v[44:45], v[28:29]\n\t"
        "v_pk_fma_f32 v[32:33], v[32:33], v[34:35], v[32:33]\n\t"
        "v_pk_fma_f32 v[36:37], v[36:37], v[38:39], v[36:37]\n\t"
        "v_pk_fma_f32 v[40:41], v[40:41], v[42:43], v[40:41]\n\t"
        "v_pk_fma_f32 v[44:45], v[44:45], v[46:47], v[44:45]\n\t"
        "v_add_u32 v92, 0x100, v92\n\t"
        "v_add_u32 v93, 0x200, v93\n\t"
        "v_add_u32 v94, 64, v94\n\t"
        "v_add_u32 v95, 0x280, v95\n\t"
        "v_pk_mul_f32 v[164:165], v[16:17], v[36:37]\n\t"
        "v_pk_mul_f32 v[166:167], v[24:25], v[44:45]\n\t"
        "v_pk_mul_f32 v[168:169], v[32:33], v[36:37]\n\t"
        "v_pk_mul_f32 v[170:171], v[40:41], v[44:45]\n\t"
        "v_pk_fma_f32 v[164:165], v[20:21], v[32:33], v[164:165]\n\t"
        "v_pk_fma_f32 v[166:167], v[28:29], v[40:41], v[166:167]\n\t"
        "v_max3_f32 v172, v172, v168, v169\n\t"
        "s_cmp_eq_u32 s28, 0\n\t"
        "v_max3_f32 v172, v172, v170, v171\n\t"
        "s_cbranch_scc0 1b\n\t"
        "v_mfma_f32_32x32x16_bf16 v[104:119], v[56:59], v[48:51], v[104:119]\n\t"
        "v_rcp_f32 v168, v168\n\t"
        "v_rcp_f32 v169, v169\n\t"
        "v_rcp_f32 v170, v170\n\t"
        "v_rcp_f32 v171, v171\n\t"
        "s_nop 0\n\t"
        "v_pk_mul_f32 v[164:165], v[164:165], v[168:169]\n\t"
        "v_pk_mul_f32 v[166:167], v[166:167], v[170:171]\n\t"
        "v_cvt_pk_bf16_f32 v54, v164, v165\n\t"
        "v_cvt_pk_bf16_f32 v55, v166, v167\n\t"
        "s_nop 1\n\t"
        "v_mfma_f32_32x32x16_bf16 v[120:135], v[56:59], v[52:55], v[120:135]\n\t"
        "s_branch 3f\n\t"
        "50:\n\t"
        "s_lshl_b32 s50, s62, 14\n\t"
        "s_lshl_b32 s51, s63, 13\n\t"
        "s_add_u32 s50, s50, s51\n\t"
        "s_add_u32 s51, s50, 4096\n\t"
        "buffer_load_dwordx4 v[212:215], v245, s[68:71], s50 offen sc1\n\t"
        "buffer_load_dwordx4 v[216:219], v245, s[68:71], s50 offen offset:1024 sc1\n\t"
        "buffer_load_dwordx4 v[220:223], v245, s[68:71], s50 offen offset:2048 sc1\n\t"
        "buffer_load_dwordx4 v[224:227], v245, s[68:71], s50 offen offset:3072 sc1\n\t"
        "buffer_load_dwordx4 v[228:231], v245, s[68:71], s51 offen sc1\n\t"
        "buffer_load_dwordx4 v[232:235], v245, s[68:71], s51 offen offset:1024 sc1\n\t"
        "buffer_load_dwordx4 v[236:239], v245, s[68:71], s51 offen offset:2048 sc1\n\t"
        "buffer_load_dwordx4 v[240:243], v245, s[68:71], s51 offen offset:3072 sc1\n\t"
        "s_mov_b32 s77, 0x7fffffff\n\t"
        "s_branch 51b\n\t"
        "2:\n\t"
        "ds_read_b128 v[56:59], v95\n\t"
        "v_rcp_f32 v168, v168\n\t"
        "v_rcp_f32 v169, v169\n\t"
        "v_rcp_f32 v170, v170\n\t"
        "v_rcp_f32 v171, v171\n\t"
        "s_nop 0\n\t"
        "v_pk_mul_f32 v[164:165], v[164:165], v[168:169]\n\t"
        "v_pk_mul_f32 v[166:167], v[166:167], v[170:171]\n\t"
        "v_cvt_pk_bf16_f32 v52, v164, v165\n\t"
        "v_cvt_pk_bf16_f32 v53, v166, v167\n\t"
        "s_waitcnt lgkmcnt(0)\n\t"
        "s_nop 0\n\t"
        "v_mfma_f32_32x32x16_bf16 v[104:119], v[56:59], v[48:51], v[104:119]\n\t"
        "v_mfma_f32_32x32x16_bf16 v[120:135], v[56:59], v[52:55], v[120:135]\n\t"
        "3:\n\t"
        "s_setprio 0\n\t"
        "s_waitcnt lgkmcnt(0)\n\t"
        "s_cmp_gt_u32 s77, 1\n\t"
        "s_cbranch_scc1 52f\n\t"
        "s_lshl_b32 s50, s62, 14\n\t"
        "s_lshl_b32 s51, s63, 13\n\t"
        "s_add_u32 s50, s50, s51\n\t"
        "s_add_u32 s51, s50, 4096\n\t"
        "buffer_load_dwordx4 v[212:215], v245, s[68:71], s50 offen sc1\n\t"
        "buffer_load_dwordx4 v[216:219], v245, s[68:71], s50 offen offset:1024 sc1\n\t"
        "buffer_load_dwordx4 v[220:223], v245, s[68:71], s50 offen offset:2048 sc1\n\t"
        "buffer_load_dwordx4 v[224:227], v245, s[68:71], s50 offen offset:3072 sc1\n\t"
        "buffer_load_dwordx4 v[228:231], v245, s[68:71], s51 offen sc1\n\t"
        "buffer_load_dwordx4 v[232:235], v245, s[68:71], s51 offen offset:1024 sc1\n\t"
        "buffer_load_dwordx4 v[236:239], v245, s[68:71], s51 offen offset:2048 sc1\n\t"
        "buffer_load_dwordx4 v[240:243], v245, s[68:71], s51 offen offset:3072 sc1\n\t"
        "s_mov_b32 s77, 0x7fffffff\n\t"
        "52:\n\t"
        "s_cmp_lg_u32 s52, 0\n\t"
        "s_cbranch_scc1 60f\n\t"
        "v_xor_b32 v81, 32, v248\n\t"
        "v_xor_b32 v82, 64, v248\n\t"
        "v_xor_b32 v83, 96, v248\n\t"
        "v_xor_b32 v84, 128, v248\n\t"
        "v_xor_b32 v85, 160, v248\n\t"
        "v_xor_b32 v86, 192, v248\n\t"
        "v_xor_b32 v87, 224, v248\n\t"
        "s_lshl_b32 s51, s53, 12\n\t"
        "s_add_u32 s51, s51, 65536\n\t"
        "ds_read_b128 v[0:3], v248\n\t"
        "ds_read_b128 v[4:7], v81\n\t"
        "ds_read_b128 v[8:11], v82\n\t"
        "ds_read_b128 v[12:15], v83\n\t"
        "ds_read_b128 v[16:19], v84\n\t"
        "ds_read_b128 v[20:23], v85\n\t"
        "ds_read_b128 v[24:27], v86\n\t"
        "ds_read_b128 v[28:31], v87\n\t"
        "v_add_u32 v64, s51, v250\n\t"
        "s_and_b32 s50, s58, 1\n\t"
        "s_lshl_b32 s50, s50, 12\n\t"
        "v_xor_b32 v65, 16, v64\n\t"
        "s_add_u32 s50, s50, s29\n\t"
        "s_xor_b32 s51, s50, 4096\n\t"
        "v_xor_b32 v66, 32, v64\n\t"
        "v_add_u32 v70, s50, v245\n\t"
        "v_add_u32 v71, s51, v245\n\t"
        "s_waitcnt vmcnt(0)\n\t"
        "v_cmp_ngt_f32 vcc, 0x7e800000, v172\n\t"
        "v_mov_b32 v47, 0x207\n\t"
        "s_mov_b64 s[34:35], vcc\n\t"
        "s_waitcnt lgkmcnt(7)\n\t"
        "v_mfma_f32_32x32x16_bf16 v[164:179], v[212:215], v[0:3], 0\n\t"
        "v_cvt_pk_bf16_f32 v32, v104, v105\n\t"
        "v_cvt_pk_bf16_f32 v33, v106, v107\n\t"
        "ds_write_b64 v64, v[32:33]\n\t"
        "v_add_f32 v44, v104, v105\n\t"
        "v_add_f32 v45, v106, v107\n\t"
        "v_add_f32 v46, v108, v109\n\t"
        "v_add_f32 v44, v110, v44\n\t"
        "s_waitcnt lgkmcnt(7)\n\t"
        "v_mfma_f32_32x32x16_bf16 v[164:179], v[216:219], v[4:7], v[164:179]\n\t"
        "v_cvt_pk_bf16_f32 v34, v108, v109\n\t"
        "v_cvt_pk_bf16_f32 v35, v110, v111\n\t"
        "ds_write_b64 v65, v[34:35]\n\t"
        "v_add_f32 v45, v111, v45\n\t"
        "v_add_f32 v46, v112, v46\n\t"
        "v_add_f32 v44, v113, v44\n\t"
        "v_add_f32 v45, v114, v45\n\t"
        "s_waitcnt lgkmcnt(7)\n\t"
        "v_mfma_f32_32x32x16_bf16 v[164:179], v[220:223], v[8:11], v[164:179]\n\t"
        "v_cvt_pk_bf16_f32 v36, v112, v113\n\t"
        "v_cvt_pk_bf16_f32 v37, v114, v115\n\t"
        "ds_write_b64 v66, v[36:37]\n\t"
        "v_add_f32 v46, v115, v46\n\t"
        "v_add_f32 v44, v120, v44\n\t"
        "v_add_f32 v45, v121, v45\n\t"
        "v_add_f32 v46, v122, v46\n\t"
        "s_waitcnt lgkmcnt(7)\n\t"
        "v_mfma_f32_32x32x16_bf16 v[164:179], v[224:227], v[12:15], v[164:179]\n\t"
        "v_cvt_pk_bf16_f32 v38, v120, v121\n\t"
        "v_cvt_pk_bf16_f32 v39, v122, v123\n\t"
        "ds_write_b64 v64, v[38:39] offset:2048\n\t"
        "v_add_f32 v44, v123, v44\n\t"
        "v_add_f32 v45, v124, v45\n\t"
        "v_add_f32 v46, v125, v46\n\t"
        "v_add_f32 v44, v126, v44\n\t"
        "s_waitcnt lgkmcnt(7)\n\t"
        "v_mfma_f32_32x32x16_bf16 v[164:179], v[228:231], v[16:19], v[164:179]\n\t"
        "v_cvt_pk_bf16_f32 v40, v124, v125\n\t"
        "v_cvt_pk_bf16_f32 v41, v126, v127\n\t"
        "ds_write_b64 v65, v[40:41] offset:2048\n\t"
        "v_add_f32 v45, v127, v45\n\t"
        "v_add_f32 v46, v128, v46\n\t"
        "v_add_f32 v44, v129, v44\n\t"
        "v_add_f32 v45, v130, v45\n\t"
        "s_waitcnt lgkmcnt(7)\n\t"
        "v_mfma_f32_32x32x16_bf16 v[164:179], v[232:235], v[20:23], v[164:179]\n\t"
        "v_cvt_pk_bf16_f32 v42, v128, v129\n\t"
        "v_cvt_pk_bf16_f32 v43, v130, v131\n\t"
        "ds_write_b64 v66, v[42:43] offset:2048\n\t"
        "v_add_f32 v46, v131, v46\n\t"
        "v_add_f32 v44, v44, v45\n\t"
        "v_add_f32 v44, v44, v46\n\t"
        "s_waitcnt lgkmcnt(7)\n\t"
        "v_mfma_f32_32x32x16_bf16 v[164:179], v[236:239], v[24:27], v[164:179]\n\t"
        "ds_read_b128 v[180:183], v70\n\t"
        "s_lshl_b32 s50, s54, 14\n\t"
        "s_add_u32 s50, s50, 65536\n\t"
        "v_add_u32 v67, s50, v251\n\t"
        "s_mul_i32 s50, s54, 5120\n\t"
        "s_lshl_b32 s51, s63, 6\n\t"
        "s_add_u32 s51, s51, s76\n\t"
        "s_add_u32 s50, s50, s51\n\t"
        "v_xor_b32 v68, 0x110, v67\n\t"
        "v_add_u32 v69, s50, v252\n\t"
        "s_waitcnt lgkmcnt(7)\n\t"
        "v_mfma_f32_32x32x16_bf16 v[164:179], v[240:243], v[28:31], v[164:179]\n\t"
        "ds_read_b128 v[184:187], v70 offset:1024\n\t"
        "ds_read_b128 v[188:191], v70 offset:2048\n\t"
        "ds_read_b128 v[192:195], v70 offset:3072\n\t"
        "ds_read_b128 v[196:199], v71\n\t"
        "ds_read_b128 v[200:203], v71 offset:1024\n\t"
        "ds_read_b128 v[204:207], v71 offset:2048\n\t"
        "ds_read_b128 v[208:211], v71 offset:3072\n\t"
        "v_cmp_class_f32 vcc, v44, v47\n\t"
        "v_mov_b32 v45, 0\n\t"
        "v_mov_b32 v46, 1\n\t"
        "s_or_b64 vcc, vcc, s[34:35]\n\t"
        "s_cbranch_scc0 62f\n\t"
        "ds_write_b32 v45, v46 offset:29384\n\t"
        "62:\n\t"
        "s_waitcnt lgkmcnt(0)\n\t"
        "s_barrier\n\t"
        "v_mov_b32 v72, 0\n\t"
        "ds_read_b32 v72, v72 offset:29384\n\t"
        "ds_read_b64_tr_b16 v[16:17], v67\n\t"
        "ds_read_b64_tr_b16 v[18:19], v68\n\t"
        "ds_read_b64_tr_b16 v[20:21], v67 offset:8192\n\t"
        "ds_read_b64_tr_b16 v[22:23], v68 offset:8192\n\t"
        "ds_read_b64_tr_b16 v[24:25], v67 offset:1024\n\t"
        "ds_read_b64_tr_b16 v[26:27], v68 offset:1024\n\t"
        "ds_read_b64_tr_b16 v[28:29], v67 offset:9216\n\t"
        "ds_read_b64_tr_b16 v[30:31], v68 offset:9216\n\t"
        "ds_read_b64_tr_b16 v[32:33], v67 offset:2048\n\t"
        "ds_read_b64_tr_b16 v[34:35], v68 offset:2048\n\t"
        "ds_read_b64_tr_b16 v[36:37], v67 offset:10240\n\t"
        "ds_read_b64_tr_b16 v[38:39], v68 offset:10240\n\t"
        "ds_read_b64_tr_b16 v[40:41], v67 offset:3072\n\t"
        "ds_read_b64_tr_b16 v[42:43], v68 offset:3072\n\t"
        "s_waitcnt lgkmcnt(14)\n\t"
        "v_readfirstlane_b32 s50, v72\n\t"
        "s_lshl_b32 s52, s50, 2\n\t"
        "s_cmp_lg_u32 s50, 0\n\t"
        "s_cbranch_scc1 63f\n\t"
        "s_waitcnt lgkmcnt(12)\n\t"
        "v_mfma_f32_32x32x16_bf16 v[0:15], v[180:183], v[16:19], 0\n\t"
        "ds_read_b64_tr_b16 v[44:45], v67 offset:11264\n\t"
        "ds_read_b64_tr_b16 v[46:47], v68 offset:11264\n\t"
        "s_waitcnt lgkmcnt(12)\n\t"
        "v_mfma_f32_32x32x16_bf16 v[0:15], v[180:183], v[20:23], v[0:15]\n\t"
        "ds_read_b64_tr_b16 v[48:49], v67 offset:4096\n\t"
        "ds_read_b64_tr_b16 v[50:51], v68 offset:4096\n\t"
        "s_waitcnt lgkmcnt(12)\n\t"
        "v_mfma_f32_32x32x16_bf16 v[0:15], v[184:187], v[24:27], v[0:15]\n\t"
        "ds_read_b64_tr_b16 v[52:53], v67 offset:12288\n\t"
        "ds_read_b64_tr_b16 v[54:55], v68 offset:12288\n\t"
        "s_waitcnt lgkmcnt(12)\n\t"
        "v_mfma_f32_32x32x16_bf16 v[0:15], v[184:187], v[28:31], v[0:15]\n\t"
        "ds_read_b64_tr_b16 v[56:57], v67 offset:5120\n\t"
        "ds_read_b64_tr_b16 v[58:59], v68 offset:5120\n\t"
        "s_waitcnt lgkmcnt(12)\n\t"
        "v_mfma_f32_32x32x16_bf16 v[0:15], v[188:191], v[32:35], v[0:15]\n\t"
        "ds_read_b64_tr_b16 v[60:61], v67 offset:13312\n\t"
        "ds_read_b64_tr_b16 v[62:63], v68 offset:13312\n\t"
        "s_waitcnt lgkmcnt(12)\n\t"
        "v_mfma_f32_32x32x16_bf16 v[0:15], v[188:191], v[36:39], v[0:15]\n\t"
        "ds_read_b64_tr_b16 v[16:17], v67 offset:6144\n\t"
        "ds_read_b64_tr_b16 v[18:19], v68 offset:6144\n\t"
        "s_waitcnt lgkmcnt(12)\n\t"
        "v_mfma_f32_32x32x16_bf16 v[0:15], v[192:195], v[40:43], v[0:15]\n\t"
        "ds_read_b64_tr_b16 v[20:21], v67 offset:14336\n\t"
        "ds_read_b64_tr_b16 v[22:23], v68 offset:14336\n\t"
        "s_waitcnt lgkmcnt(12)\n\t"
        "v_mfma_f32_32x32x16_bf16 v[0:15], v[192:195], v[44:47], v[0:15]\n\t"
        "ds_read_b64_tr_b16 v[24:25], v67 offset:7168\n\t"
        "ds_read_b64_tr_b16 v[26:27], v68 offset:7168\n\t"
        "s_waitcnt lgkmcnt(12)\n\t"
        "v_mfma_f32_32x32x16_bf16 v[0:15], v[196:199], v[48:51], v[0:15]\n\t"
        "ds_read_b64_tr_b16 v[28:29], v67 offset:15360\n\t"
        "ds_read_b64_tr_b16 v[30:31], v68 offset:15360\n\t"
        "s_waitcnt lgkmcnt(12)\n\t"
        "v_mfma_f32_32x32x16_bf16 v[0:15], v[196:199], v[52:55], v[0:15]\n\t"
        "s_waitcnt lgkmcnt(10)\n\t"
        "v_mfma_f32_32x32x16_bf16 v[0:15], v[200:203], v[56:59], v[0:15]\n\t"
        "s_waitcnt lgkmcnt(8)\n\t"
        "v_mfma_f32_32x32x16_bf16 v[0:15], v[200:203], v[60:63], v[0:15]\n\t"
        "s_waitcnt lgkmcnt(6)\n\t"
        "v_mfma_f32_32x32x16_bf16 v[0:15], v[204:207], v[16:19], v[0:15]\n\t"
        "s_waitcnt lgkmcnt(4)\n\t"
        "v_mfma_f32_32x32x16_bf16 v[0:15], v[204:207], v[20:23], v[0:15]\n\t"
        "s_waitcnt lgkmcnt(2)\n\t"
        "v_mfma_f32_32x32x16_bf16 v[0:15], v[208:211], v[24:27], v[0:15]\n\t"
        "s_waitcnt lgkmcnt(0)\n\t"
        "v_mfma_f32_32x32x16_bf16 v[0:15], v[208:211], v[28:31], v[0:15]\n\t"
        "s_mov_b32 s50, 0xbfb8aa3b\n\t"
        "s_mov_b32 s32, 0xbf317218\n\t"
        "s_mov_b32 s33, 0xbf317218\n\t"
        "s_nop 8\n\t"
        "s_mov_b32 exec_lo, 0xfffff\n\t"
        "s_mov_b32 exec_hi, 0xfffff\n\t"
        "v_fmac_f32 v0, s50, v164\n\t"
        "v_fmac_f32 v1, s50, v165\n\t"
        "v_fmac_f32 v2, s50, v166\n\t"
        "v_fmac_f32 v3, s50, v167\n\t"
        "v_fmac_f32 v4, s50, v168\n\t"
        "v_fmac_f32 v5, s50, v169\n\t"
        "v_fmac_f32 v6, s50, v170\n\t"
        "v_fmac_f32 v7, s50, v171\n\t"
        "v_fmac_f32 v8, s50, v172\n\t"
        "v_fmac_f32 v9, s50, v173\n\t"
        "v_fmac_f32 v10, s50, v174\n\t"
        "v_fmac_f32 v11, s50, v175\n\t"
        "v_fmac_f32 v12, s50, v176\n\t"
        "v_fmac_f32 v13, s50, v177\n\t"
        "v_fmac_f32 v14, s50, v178\n\t"
        "v_fmac_f32 v15, s50, v179\n\t"
        "v_exp_f32 v16, v0\n\t"
        "v_exp_f32 v17, v1\n\t"
        "v_exp_f32 v18, v2\n\t"
        "v_exp_f32 v19, v3\n\t"
        "v_exp_f32 v20, v4\n\t"
        "v_exp_f32 v21, v5\n\t"
        "v_exp_f32 v22, v6\n\t"
        "v_exp_f32 v23, v7\n\t"
        "v_exp_f32 v24, v8\n\t"
        "v_exp_f32 v25, v9\n\t"
        "v_exp_f32 v26, v10\n\t"
        "v_exp_f32 v27, v11\n\t"
        "v_exp_f32 v28, v12\n\t"
        "v_exp_f32 v29, v13\n\t"
        "v_exp_f32 v30, v14\n\t"
        "v_exp_f32 v31, v15\n\t"
        "v_pk_add_f32 v[16:17], v[16:17], 1.0 op_sel_hi:[1,0]\n\t"
        "v_pk_add_f32 v[18:19], v[18:19], 1.0 op_sel_hi:[1,0]\n\t"
        "v_pk_add_f32 v[20:21], v[20:21], 1.0 op_sel_hi:[1,0]\n\t"
        "v_pk_add_f32 v[22:23], v[22:23], 1.0 op_sel_hi:[1,0]\n\t"
        "v_pk_add_f32 v[24:25], v[24:25], 1.0 op_sel_hi:[1,0]\n\t"
        "v_pk_add_f32 v[26:27], v[26:27], 1.0 op_sel_hi:[1,0]\n\t"
        "v_pk_add_f32 v[28:29], v[28:29], 1.0 op_sel_hi:[1,0]\n\t"
        "v_pk_add_f32 v[30:31], v[30:31], 1.0 op_sel_hi:[1,0]\n\t"
        "v_rcp_f32 v16, v16\n\t"
        "v_rcp_f32 v17, v17\n\t"
        "v_rcp_f32 v18, v18\n\t"
        "v_rcp_f32 v19, v19\n\t"
        "v_rcp_f32 v20, v20\n\t"
        "v_rcp_f32 v21, v21\n\t"
        "v_rcp_f32 v22, v22\n\t"
        "v_rcp_f32 v23, v23\n\t"
        "v_rcp_f32 v24, v24\n\t"
        "v_rcp_f32 v25, v25\n\t"
        "v_rcp_f32 v26, v26\n\t"
        "v_rcp_f32 v27, v27\n\t"
        "v_rcp_f32 v28, v28\n\t"
        "v_rcp_f32 v29, v29\n\t"
        "v_rcp_f32 v30, v30\n\t"
        "v_rcp_f32 v31, v31\n\t"
        "v_pk_mul_f32 v[0:1], v[0:1], s[32:33]\n\t"
        "v_pk_mul_f32 v[2:3], v[2:3], s[32:33]\n\t"
        "v_pk_mul_f32 v[4:5], v[4:5], s[32:33]\n\t"
        "v_pk_mul_f32 v[6:7], v[6:7], s[32:33]\n\t"
        "v_pk_mul_f32 v[8:9], v[8:9], s[32:33]\n\t"
        "v_pk_mul_f32 v[10:11], v[10:11], s[32:33]\n\t"
        "v_pk_mul_f32 v[12:13], v[12:13], s[32:33]\n\t"
        "v_pk_mul_f32 v[14:15], v[14:15], s[32:33]\n\t"
        "v_pk_mul_f32 v[0:1], v[0:1], v[16:17]\n\t"
        "v_pk_mul_f32 v[2:3], v[2:3], v[18:19]\n\t"
        "v_pk_mul_f32 v[4:5], v[4:5], v[20:21]\n\t"
        "v_pk_mul_f32 v[6:7], v[6:7], v[22:23]\n\t"
        "v_pk_mul_f32 v[8:9], v[8:9], v[24:25]\n\t"
        "v_pk_mul_f32 v[10:11], v[10:11], v[26:27]\n\t"
        "v_pk_mul_f32 v[12:13], v[12:13], v[28:29]\n\t"
        "v_pk_mul_f32 v[14:15], v[14:15], v[30:31]\n\t"
        "v_cvt_pk_bf16_f32 v32, v0, v1\n\t"
        "v_cvt_pk_bf16_f32 v33, v2, v3\n\t"
        "v_cvt_pk_bf16_f32 v34, v4, v5\n\t"
        "v_cvt_pk_bf16_f32 v35, v6, v7\n\t"
        "v_cvt_pk_bf16_f32 v36, v8, v9\n\t"
        "v_cvt_pk_bf16_f32 v37, v10, v11\n\t"
        "v_cvt_pk_bf16_f32 v38, v12, v13\n\t"
        "v_cvt_pk_bf16_f32 v39, v14, v15\n\t"
        "buffer_store_dwordx4 v[32:35], v69, s[72:75], s59 offen\n\t"
        "buffer_store_dwordx4 v[36:39], v69, s[72:75], s59 offen offset:16\n\t"
        "s_mov_b64 exec, -1\n\t"
        "s_nop 1\n\t"
        "s_branch 61f\n\t"
        "60:\n\t"
        "s_nop 15\n\t"
        "v_cmp_ngt_f32 vcc, 0x7e800000, v172\n\t"
        "v_mov_b32 v47, 0x207\n\t"
        "s_mov_b64 s[34:35], vcc\n\t"
        "v_add_f32 v44, v104, v105\n\t"
        "v_add_f32 v45, v106, v107\n\t"
        "v_add_f32 v46, v108, v109\n\t"
        "v_add_f32 v44, v110, v44\n\t"
        "v_add_f32 v45, v111, v45\n\t"
        "v_add_f32 v46, v112, v46\n\t"
        "v_add_f32 v44, v113, v44\n\t"
        "v_add_f32 v45, v114, v45\n\t"
        "v_add_f32 v46, v115, v46\n\t"
        "v_add_f32 v44, v120, v44\n\t"
        "v_add_f32 v45, v121, v45\n\t"
        "v_add_f32 v46, v122, v46\n\t"
        "v_add_f32 v44, v123, v44\n\t"
        "v_add_f32 v45, v124, v45\n\t"
        "v_add_f32 v46, v125, v46\n\t"
        "v_add_f32 v44, v126, v44\n\t"
        "v_add_f32 v45, v127, v45\n\t"
        "v_add_f32 v46, v128, v46\n\t"
        "v_add_f32 v44, v129, v44\n\t"
        "v_add_f32 v45, v130, v45\n\t"
        "v_add_f32 v46, v131, v46\n\t"
        "v_add_f32 v44, v44, v45\n\t"
        "v_add_f32 v44, v44, v46\n\t"
        "v_cmp_class_f32 vcc, v44, v47\n\t"
        "v_mov_b32 v45, 0\n\t"
        "v_mov_b32 v46, 1\n\t"
        "s_or_b64 vcc, vcc, s[34:35]\n\t"
        "s_cbranch_scc0 64f\n\t"
        "ds_write_b32 v45, v46 offset:29384\n\t"
        "64:\n\t"
        "s_waitcnt lgkmcnt(0)\n\t"
        "s_barrier\n\t"
        "v_mov_b32 v72, 0\n\t"
        "ds_read_b32 v72, v72 offset:29384\n\t"
        "s_waitcnt lgkmcnt(0)\n\t"
        "v_readfirstlane_b32 s50, v72\n\t"
        "s_lshl_b32 s50, s50, 2\n\t"
        "s_or_b32 s52, s52, s50\n\t"
        "63:\n\t"
        "s_waitcnt vmcnt(0) lgkmcnt(0)\n\t"
        "s_nop 15\n\t"
        "61:\n\t"
        "s_mov_b32 m0, s31\n\t"
      : "={v[104:119]}"(agg0), "={v[120:135]}"(agg1), "={v[136:151]}"(w3), "={v[152:159]}"(ident),
        "={v[212:227]}"(wua), "={v[228:243]}"(wub), "={v[248:255]}"(epi), "={s52}"(more), "={s58}"(nlast), "={s79}"(tag)
      : "{v244}"(tid0), "{s[0:1]}"(ka), "{s78}"(b), "{s[100:101]}"(lane_tab)
      : "v0", "v1", "v2", "v3", "v4", "v5", "v6", "v7", "v8", "v9", "v10", "v11", "v12", "v13", "v14", "v15", "v16", "v17", "v18", "v19", "v20", "v21", "v22", "v23", "v24", "v25", "v26", "v27", "v28", "v29", "v30", "v31", "v32", "v33", "v34", "v35", "v36", "v37", "v38", "v39", "v40", "v41", "v42", "v43", "v44", "v45", "v46", "v47", "v48", "v49", "v50", "v51", "v52", "v53", "v54", "v55", "v56", "v57", "v58", "v59", "v60", "v61", "v62", "v63", "v64", "v65", "v66", "v67", "v68", "v69", "v70", "v71", "v72", "v73", "v74", "v75", "v76", "v77", "v78", "v79", "v80", "v81", "v82", "v83", "v84", "v85", "v86", "v87", "v88", "v89", "v90", "v91", "v92", "v93", "v94", "v95", "v96", "v97", "v98", "v99", "v100", "v101", "v102", "v103", "v160", "v161", "v162", "v163", "v164", "v165", "v166", "v167", "v168", "v169", "v170", "v171", "v172", "v173", "v174", "v175", "v176", "v177", "v178", "v179", "v180", "v181", "v182", "v183", "v184", "v185", "v186", "v187", "v188", "v189", "v190", "v191", "v192", "v193", "v194", "v195", "v196", "v197", "v198", "v199", "v200", "v201", "v202", "v203", "v204", "v205", "v206", "v207", "v208", "v209", "v210", "v211", "v245", "v246", "v247", "s2", "s3", "s4", "s5", "s6", "s7", "s8", "s9", "s10", "s11", "s12", "s13", "s14", "s15", "s16", "s17", "s18", "s19", "s20", "s21", "s22", "s23", "s24", "s25", "s26", "s27", "s28", "s29", "s30", "s31", "s32", "s33", "s34", "s35", "s36", "s37", "s38", "s39", "s40", "s41", "s42", "s43", "s44", "s45", "s46", "s47", "s48", "s49", "s50", "s51", "s53", "s54", "s55", "s56", "s57", "s59", "s60", "s61", "s62", "s63", "s64", "s65", "s66", "s67", "s68", "s69", "s70", "s71", "s72", "s73", "s74", "s75", "s76", "s77", "s80", "s81", "s82", "s83", "s84", "s85", "s86", "s87", "s88", "s89", "s90", "s91", "s92", "s93", "s94", "s95", "s96", "s97", "s98", "s99", "vcc", "scc", "memory");
  char* ws = ka->ws;
  if (b == 0 && tid0 == 0) __hip_atomic_store((u32*)ws, tag, __ATOMIC_RELAXED, __HIP_MEMORY_SCOPE_AGENT);
  if (more) {
    const u32* slots = (const u32*)(ws + WS_SLOT) + b * (CH * CAP);
    const u32 bb = b;
    const int w = __builtin_amdgcn_readfirstlane(tid0 >> 6);
    const u32 m0 = bucket_start(bb) * 256;
    const u32x4 sp = make_srd(ws + WS_TAB - 3072, 256, TROWS), sf = make_srd(ka->ea, 32, NE), st = make_srd(lane_tab, 0, 12288);
    if (more & 4) {
      float z = 0.f;
      PIN(z);
#pragma unroll
      for (int i = 0; i < 16; ++i) { agg0[i] = z; agg1[i] = z; }
    }
    int mode = (more & 5) ? 1 : 2, cur = 0, curo = 0;
    for (;;) {
      u32 e0 = ~0u, cbase;
      if (mode == 1) {
        const int i = cur * 512 + tid0;
        e0 = ld_dev(slots + i);
        cbase = (((i >> 5) - bb) & 127) * CE;
      } else {
        u32 n = 0;
        while (cur < CH) {
          n = __builtin_amdgcn_readfirstlane(ld_dev((const u32*)(ws + F_OCNT) + cur));
          if ((u32)curo < n) break;
          ++cur; curo = 0;
        }
        if (cur == CH) break;
        if ((u32)(curo + tid0) < n) {
          const unsigned long long v = ld_dev((const unsigned long long*)(ws + WS_OVF) + cur * CE + curo + tid0);
          if ((u32)(v >> 32) == b) e0 = (u32)v;
        }
        cbase = cur * CE;
      }
      u32x8 en = {e0, ~0u, ~0u, ~0u, ~0u, ~0u, ~0u, ~0u};
      PIN(en); PIN(cbase);
      u32x16 sc = cat4(st, st, st, st);
      asm volatile("s_barrier" ::: "memory");
      asm volatile(
        "s_mov_b32 s31, m0\n\t"
        "s_lshr_b32 s54, s53, 2\n\t"
        "s_lshr_b32 s56, s53, 1\n\t"
        "s_and_b32 s62, s53, 1\n\t"
        "s_and_b32 s63, s56, 1\n\t"
        "s_xor_b32 s55, s63, s54\n\t"
        "s_xor_b32 s57, s62, s63\n\t"
        "s_lshl_b32 s76, s62, 7\n\t"
        "s_lshl_b32 s29, s53, 13\n\t"
        "s_lshl_b32 s30, s53, 12\n\t"
        "s_add_u32 s29, s29, 98304\n\t"
        "s_add_u32 s30, s30, 65536\n\t"
        "v_and_b32 v245, 63, v244\n\t"
        "v_lshlrev_b32 v245, 4, v245\n\t"
        "s_lshl_b32 s50, s53, 10\n\t"
        "s_add_u32 m0, s30, 2048\n\t"
        "s_nop 0\n\t"
        "buffer_load_dwordx4 v245, s[36:39], s50 offen lds\n\t"
        "s_cmp_ge_u32 s53, 4\n\t"
        "s_cbranch_scc1 31f\n\t"
        "s_add_u32 m0, s30, 1024\n\t"
        "s_add_u32 s50, s50, 8192\n\t"
        "buffer_load_dwordx4 v245, s[36:39], s50 offen lds\n\t"
        "31:\n\t"
        "v_add_u32 v246, 65536, v245\n\t"
        "v_lshlrev_b32 v0, 4, v244\n\t"
        "v_mov_b64 v[4:5], 0\n\t"
        "v_mov_b64 v[6:7], 0\n\t"
        "v_mov_b64 v[8:9], -1\n\t"
        "v_mov_b64 v[10:11], -1\n\t"
        "v_bfe_u32 v1, v244, 1, 1\n\t"
        "v_bfe_i32 v2, v244, 2, 1\n\t"
        "v_lshlrev_b32 v1, 4, v1\n\t"
        "v_mov_b32 v3, 0x3f80\n\t"
        "ds_write_b128 v0, v[4:7] offset:10240\n\t"
        "v_lshlrev_b32 v1, v1, v3\n\t"
        "ds_write_b128 v0, v[4:7] offset:18432\n\t"
        "v_and_b32 v13, v1, v2\n\t"
        "ds_write_b128 v0, v[8:11] offset:30720\n\t"
        "v_xor_b32 v12, v1, v13\n\t"
        "v_mov_b32 v15, v13\n\t"
        "v_mov_b32 v14, v12\n\t"
        "ds_write_b128 v0, v[12:15] offset:41984\n\t"
        "s_cmp_lt_u32 s53, 5\n\t"
        "s_cselect_b64 exec, -1, 0\n\t"
        "ds_write_b128 v0, v[12:15] offset:50176\n\t"
        "s_cmp_lt_u32 s53, 4\n\t"
        "s_cselect_b64 exec, -1, 0\n\t"
        "ds_write_b128 v0, v[4:7] offset:26624\n\t"
        "s_cmp_lt_u32 s53, 3\n\t"
        "s_cselect_b64 exec, -1, 0\n\t"
        "ds_write_b128 v0, v[8:11] offset:38912\n\t"
        "s_mov_b64 exec, -1\n\t"
        "v_mov_b32 v40, 1\n\t"
        "s_and_b32 s49, s53, 3\n\t"
        "s_mul_i32 s49, s49, 5\n\t"
        "s_lshr_b32 s58, s59, 8\n\t"
        "s_waitcnt vmcnt(0) lgkmcnt(0)\n\t"
        "s_barrier\n\t"
        "ds_read_b128 v[48:51], v246 offset:2048\n\t"
        "ds_read_b128 v[192:195], v246 offset:6144\n\t"
        "ds_read_b128 v[80:83], v246 offset:10240\n\t"
        "ds_read_b128 v[84:87], v246 offset:14336\n\t"
        "ds_read_b128 v[88:91], v246 offset:18432\n\t"
        "ds_read_b128 v[92:95], v246 offset:22528\n\t"
        "ds_read_b128 v[96:99], v246 offset:26624\n\t"
        "ds_read_b128 v[100:103], v246 offset:30720\n\t"
        "s_waitcnt lgkmcnt(0)\n\t"
        "v_add_u32 v190, s49, v50\n\t"
        "v_mov_b32 v191, v51\n\t"
        "v_add_u32 v195, s58, v195\n\t"
        "s_mul_i32 s50, s54, 20\n\t"
        "s_add_u32 s50, s50, s58\n\t"
        "v_lshlrev_b32 v195, 2, v195\n\t"
        "v_add_u32 v198, s50, v190\n\t"
        "v_and_b32 v189, 0xfc, v195\n\t"
        "v_lshlrev_b32 v198, 2, v198\n\t"
        "v_and_b32 v198, 0xfc, v198\n\t"
        "s_mul_i32 s50, s54, 30\n\t"
        "s_add_u32 s50, s50, s55\n\t"
        "s_lshl_b32 s51, s50, 6\n\t"
        "s_add_u32 s51, s51, 30720\n\t"
        "v_add_u32 v92, s51, v92\n\t"
        "s_lshl_b32 s51, s50, 7\n\t"
        "s_add_u32 s51, s51, 34560\n\t"
        "v_add_u32 v93, s51, v93\n\t"
        "s_lshl_b32 s51, s50, 4\n\t"
        "s_add_u32 s51, s51, 10240\n\t"
        "v_add_u32 v94, s51, v94\n\t"
        "s_lshl_b32 s50, s54, 1\n\t"
        "s_add_u32 s50, s50, s63\n\t"
        "s_mul_i32 s50, s50, 4480\n\t"
        "s_add_u32 s50, s50, 11200\n\t"
        "v_add_u32 v96, s29, v96\n\t"
        "v_add_u32 v97, s29, v97\n\t"
        "v_add_u32 v98, s29, v98\n\t"
        "v_add_u32 v99, s29, v99\n\t"
        "v_add_u32 v95, s50, v95\n\t"
        "v_add_u32 v100, s30, v100\n\t"
        "v_or_b32 v101, s76, v101\n\t"
        "s_mul_i32 s50, s54, 26\n\t"
        "s_add_u32 s50, s50, s55\n\t"
        "s_lshl_b32 s50, s50, 8\n\t"
        "s_add_u32 s50, s50, 41984\n\t"
        "v_cmp_ne_u32 vcc, 0, v103\n\t"
        "v_add_u32 v102, s50, v102\n\t"
        "v_mov_b32 v199, 29392\n\t"
        "s_nop 0\n\t"
        "v_cndmask_b32 v102, v199, v102, vcc\n\t"
        "v_mov_b32 v65, v81\n\t"
        "v_mov_b32 v67, v83\n\t"
        "v_mov_b32 v69, v85\n\t"
        "v_mov_b32 v71, v87\n\t"
        "v_mov_b32 v73, v89\n\t"
        "v_mov_b32 v75, v89\n\t"
        "v_mov_b64 v[162:163], 0\n\t"
        "v_lshlrev_b32 v0, 2, v180\n\t"
        "v_cmp_ne_u32 s[32:33], -1, v180\n\t"
        "v_lshlrev_b32 v1, 2, v181\n\t"
        "v_cmp_ne_u32 s[34:35], -1, v181\n\t"
        "v_lshlrev_b32 v2, 2, v182\n\t"
        "v_cmp_ne_u32 s[36:37], -1, v182\n\t"
        "v_lshlrev_b32 v3, 2, v183\n\t"
        "v_cmp_ne_u32 s[38:39], -1, v183\n\t"
        "v_and_b32 v0, 0xfc, v0\n\t"
        "v_and_b32 v1, 0xfc, v1\n\t"
        "v_and_b32 v2, 0xfc, v2\n\t"
        "v_and_b32 v3, 0xfc, v3\n\t"
        "s_mov_b64 exec, s[32:33]\n\t"
        "ds_add_rtn_u32 v8, v0, v40 offset:29120\n\t"
        "s_mov_b64 exec, s[34:35]\n\t"
        "ds_add_rtn_u32 v9, v1, v40 offset:29120\n\t"
        "s_mov_b64 exec, s[36:37]\n\t"
        "ds_add_rtn_u32 v10, v2, v40 offset:29120\n\t"
        "s_mov_b64 exec, s[38:39]\n\t"
        "ds_add_rtn_u32 v11, v3, v40 offset:29120\n\t"
        "s_mov_b64 exec, -1\n\t"
        "v_bfe_u32 v16, v180, 6, 14\n\t"
        "v_lshrrev_b32 v24, 20, v180\n\t"
        "v_bfe_u32 v17, v181, 6, 14\n\t"
        "v_lshrrev_b32 v25, 20, v181\n\t"
        "v_bfe_u32 v18, v182, 6, 14\n\t"
        "v_lshrrev_b32 v26, 20, v182\n\t"
        "v_bfe_u32 v19, v183, 6, 14\n\t"
        "v_lshrrev_b32 v27, 20, v183\n\t"
        "v_add_u32 v24, v24, v188\n\t"
        "v_add_u32 v25, v25, v188\n\t"
        "v_add_u32 v26, v26, v188\n\t"
        "v_add_u32 v27, v27, v188\n\t"
        "s_mov_b32 s50, 0x88888888\n\t"
        "s_mov_b32 s51, 0x88888888\n\t"
        "s_and_b64 s[40:41], s[38:39], s[50:51]\n\t"
        "s_cbranch_scc0 10f\n\t"
        "s_lshr_b64 s[50:51], s[40:41], 1\n\t"
        "s_or_b64 s[50:51], s[50:51], s[40:41]\n\t"
        "s_lshr_b64 s[42:43], s[50:51], 2\n\t"
        "s_or_b64 s[50:51], s[50:51], s[42:43]\n\t"
        "v_lshlrev_b32 v4, 2, v184\n\t"
        "v_cmp_ne_u32 s[40:41], -1, v184\n\t"
        "v_lshlrev_b32 v5, 2, v185\n\t"
        "v_cmp_ne_u32 s[42:43], -1, v185\n\t"
        "v_lshlrev_b32 v6, 2, v186\n\t"
        "v_cmp_ne_u32 s[44:45], -1, v186\n\t"
        "v_lshlrev_b32 v7, 2, v187\n\t"
        "v_cmp_ne_u32 s[46:47], -1, v187\n\t"
        "v_and_b32 v4, 0xfc, v4\n\t"
        "v_and_b32 v5, 0xfc, v5\n\t"
        "v_and_b32 v6, 0xfc, v6\n\t"
        "v_and_b32 v7, 0xfc, v7\n\t"
        "s_and_b64 s[40:41], s[40:41], s[50:51]\n\t"
        "s_and_b64 s[42:43], s[42:43], s[50:51]\n\t"
        "s_and_b64 s[44:45], s[44:45], s[50:51]\n\t"
        "s_and_b64 s[46:47], s[46:47], s[50:51]\n\t"
        "s_mov_b64 exec, s[40:41]\n\t"
        "ds_add_rtn_u32 v12, v4, v40 offset:29120\n\t"
        "s_mov_b64 exec, s[42:43]\n\t"
        "ds_add_rtn_u32 v13, v5, v40 offset:29120\n\t"
        "s_mov_b64 exec, s[44:45]\n\t"
        "ds_add_rtn_u32 v14, v6, v40 offset:29120\n\t"
        "s_mov_b64 exec, s[46:47]\n\t"
        "ds_add_rtn_u32 v15, v7, v40 offset:29120\n\t"
        "s_mov_b64 exec, -1\n\t"
        "v_bfe_u32 v20, v184, 6, 14\n\t"
        "v_lshrrev_b32 v28, 20, v184\n\t"
        "v_bfe_u32 v21, v185, 6, 14\n\t"
        "v_lshrrev_b32 v29, 20, v185\n\t"
        "v_bfe_u32 v22, v186, 6, 14\n\t"
        "v_lshrrev_b32 v30, 20, v186\n\t"
        "v_bfe_u32 v23, v187, 6, 14\n\t"
        "v_lshrrev_b32 v31, 20, v187\n\t"
        "v_add_u32 v28, v28, v188\n\t"
        "v_add_u32 v29, v29, v188\n\t"
        "v_add_u32 v30, v30, v188\n\t"
        "v_add_u32 v31, v31, v188\n\t"
        "s_mov_b32 s50, 0x88888888\n\t"
        "s_mov_b32 s51, 0x88888888\n\t"
        "s_and_b64 exec, s[46:47], s[50:51]\n\t"
        "s_cbranch_scc0 11f\n\t"
        "v_mov_b32 v41, 0\n\t"
        "ds_write_b32 v41, v40 offset:29376\n\t"
        "11:\n\t"
        "s_mov_b64 exec, -1\n\t"
        "10:\n\t"
        "s_waitcnt lgkmcnt(0)\n\t"
        "s_barrier\n\t"
        "ds_read_b32 v41, v189 offset:29120\n\t"
        "ds_read_b32 v196, v198 offset:29120\n\t"
        "s_waitcnt lgkmcnt(1)\n\t"
        "v_add_u32 v42, 3, v41\n\t"
        "v_lshrrev_b32 v42, 2, v42\n\t"
        "s_nop 1\n\t"
        "v_add_u32_dpp v43, v42, v42 row_shr:1 row_mask:0xf bank_mask:0xf bound_ctrl:1\n\t"
        "s_nop 1\n\t"
        "v_add_u32_dpp v43, v43, v43 row_shr:2 row_mask:0xf bank_mask:0xf bound_ctrl:1\n\t"
        "s_nop 1\n\t"
        "v_add_u32_dpp v43, v43, v43 row_shr:4 row_mask:0xf bank_mask:0xf bound_ctrl:1\n\t"
        "s_nop 1\n\t"
        "v_add_u32_dpp v43, v43, v43 row_shr:8 row_mask:0xf bank_mask:0xf bound_ctrl:1\n\t"
        "s_nop 1\n\t"
        "v_add_u32_dpp v43, v43, v43 row_bcast:15 row_mask:0xa bank_mask:0xf\n\t"
        "v_sub_u32 v44, v43, v42\n\t"
        "v_readlane_b32 s48, v43, 31\n\t"
        "v_readlane_b32 s49, v43, 63\n\t"
        "v_lshl_add_u32 v44, v44, 2, v192\n\t"
        "ds_write_b32 v189, v44 offset:29400\n\t"
        "ds_read_b32 v32, v0 offset:29400\n\t"
        "ds_read_b32 v33, v1 offset:29400\n\t"
        "ds_read_b32 v34, v2 offset:29400\n\t"
        "ds_read_b32 v35, v3 offset:29400\n\t"
        "ds_read_b32 v197, v198 offset:29400\n\t"
        "s_max_u32 s50, s48, s49\n\t"
        "s_mov_b32 s28, 0\n\t"
        "s_cmp_le_u32 s50, 208\n\t"
        "s_cselect_b32 s52, 0, 1\n\t"
        "s_cbranch_scc0 20f\n\t"
        "s_waitcnt lgkmcnt(4)\n\t"
        "v_add_u32 v32, v32, v8\n\t"
        "v_lshlrev_b32 v8, 1, v32\n\t"
        "v_lshlrev_b32 v32, 2, v32\n\t"
        "s_waitcnt lgkmcnt(3)\n\t"
        "v_add_u32 v33, v33, v9\n\t"
        "v_lshlrev_b32 v9, 1, v33\n\t"
        "v_lshlrev_b32 v33, 2, v33\n\t"
        "s_waitcnt lgkmcnt(2)\n\t"
        "v_add_u32 v34, v34, v10\n\t"
        "v_lshlrev_b32 v10, 1, v34\n\t"
        "v_lshlrev_b32 v34, 2, v34\n\t"
        "s_waitcnt lgkmcnt(1)\n\t"
        "v_add_u32 v35, v35, v11\n\t"
        "v_lshlrev_b32 v11, 1, v35\n\t"
        "v_lshlrev_b32 v35, 2, v35\n\t"
        "s_mov_b64 exec, s[32:33]\n\t"
        "ds_write_b16 v8, v16 offset:30720\n\t"
        "ds_write_b32 v32, v24 offset:34560\n\t"
        "s_mov_b64 exec, s[34:35]\n\t"
        "ds_write_b16 v9, v17 offset:30720\n\t"
        "ds_write_b32 v33, v25 offset:34560\n\t"
        "s_mov_b64 exec, s[36:37]\n\t"
        "ds_write_b16 v10, v18 offset:30720\n\t"
        "ds_write_b32 v34, v26 offset:34560\n\t"
        "s_mov_b64 exec, s[38:39]\n\t"
        "ds_write_b16 v11, v19 offset:30720\n\t"
        "ds_write_b32 v35, v27 offset:34560\n\t"
        "s_mov_b64 exec, -1\n\t"
        "s_cmp_eq_u64 s[40:41], 0\n\t"
        "s_cbranch_scc1 12f\n\t"
        "ds_read_b32 v36, v4 offset:29400\n\t"
        "ds_read_b32 v37, v5 offset:29400\n\t"
        "ds_read_b32 v38, v6 offset:29400\n\t"
        "ds_read_b32 v39, v7 offset:29400\n\t"
        "s_waitcnt lgkmcnt(3)\n\t"
        "v_add_u32 v36, v36, v12\n\t"
        "v_lshlrev_b32 v12, 1, v36\n\t"
        "v_lshlrev_b32 v36, 2, v36\n\t"
        "s_waitcnt lgkmcnt(2)\n\t"
        "v_add_u32 v37, v37, v13\n\t"
        "v_lshlrev_b32 v13, 1, v37\n\t"
        "v_lshlrev_b32 v37, 2, v37\n\t"
        "s_waitcnt lgkmcnt(1)\n\t"
        "v_add_u32 v38, v38, v14\n\t"
        "v_lshlrev_b32 v14, 1, v38\n\t"
        "v_lshlrev_b32 v38, 2, v38\n\t"
        "s_waitcnt lgkmcnt(0)\n\t"
        "v_add_u32 v39, v39, v15\n\t"
        "v_lshlrev_b32 v15, 1, v39\n\t"
        "v_lshlrev_b32 v39, 2, v39\n\t"
        "s_mov_b64 exec, s[40:41]\n\t"
        "ds_write_b16 v12, v20 offset:30720\n\t"
        "ds_write_b32 v36, v28 offset:34560\n\t"
        "s_mov_b64 exec, s[42:43]\n\t"
        "ds_write_b16 v13, v21 offset:30720\n\t"
        "ds_write_b32 v37, v29 offset:34560\n\t"
        "s_mov_b64 exec, s[44:45]\n\t"
        "ds_write_b16 v14, v22 offset:30720\n\t"
        "ds_write_b32 v38, v30 offset:34560\n\t"
        "s_mov_b64 exec, s[46:47]\n\t"
        "ds_write_b16 v15, v23 offset:30720\n\t"
        "ds_write_b32 v39, v31 offset:34560\n\t"
        "s_mov_b64 exec, -1\n\t"
        "12:\n\t"
        "s_cmp_ge_u32 s53, 4\n\t"
        "s_cbranch_scc1 16f\n\t"
        "s_cmp_eq_u32 s56, 0\n\t"
        "s_cselect_b32 s50, s48, s49\n\t"
        "s_add_u32 s50, s50, 15\n\t"
        "s_lshr_b32 s50, s50, 3\n\t"
        "s_sub_u32 s50, s50, s57\n\t"
        "s_lshr_b32 s50, s50, 1\n\t"
        "s_mul_i32 s51, s56, 30\n\t"
        "s_lshl_b32 s50, s50, 1\n\t"
        "s_add_u32 s51, s51, s57\n\t"
        "s_add_u32 s50, s50, s51\n\t"
        "s_lshl_b32 s50, s50, 6\n\t"
        "s_lshl_b32 s51, s62, 6\n\t"
        "v_add_u32 v51, s50, v193\n\t"
        "v_add_u32 v52, s51, v194\n\t"
        "ds_write_b16 v51, v52 offset:30720\n\t"
        "16:\n\t"
        "s_cmp_eq_u32 s54, 0\n\t"
        "s_cselect_b32 s28, s48, s49\n\t"
        "s_add_u32 s28, s28, 15\n\t"
        "s_lshr_b32 s28, s28, 3\n\t"
        "s_sub_u32 s28, s28, s55\n\t"
        "s_lshr_b32 s28, s28, 1\n\t"
        "20:\n\t"
        "s_waitcnt lgkmcnt(0)\n\t"
        "s_barrier\n\t"
        "s_cmp_ge_u32 s53, 4\n\t"
        "s_cbranch_scc1 70f\n\t"
        "ds_read_u16 v80, v92\n\t"
        "ds_read_u16 v82, v92 offset:16\n\t"
        "ds_read_u16 v84, v92 offset:32\n\t"
        "ds_read_u16 v86, v92 offset:48\n\t"
        "ds_read_b32 v88, v93\n\t"
        "s_cmp_gt_u32 s28, 0\n\t"
        "s_cselect_b32 s26, 320000, 0\n\t"
        "s_mov_b32 m0, s30\n\t"
        "s_waitcnt lgkmcnt(0)\n\t"
        "buffer_load_dwordx4 v[88:89], s[24:27], 0 idxen offen lds\n\t"
        "s_mov_b32 m0, s29\n\t"
        "s_nop 0\n\t"
        "buffer_load_dwordx4 v[80:81], s[20:23], s76 idxen offen sc1 lds\n\t"
        "buffer_load_dwordx4 v[82:83], s[20:23], s76 idxen offen offset:1024 sc1 lds\n\t"
        "buffer_load_dwordx4 v[84:85], s[20:23], s76 idxen offen offset:2048 sc1 lds\n\t"
        "buffer_load_dwordx4 v[86:87], s[20:23], s76 idxen offen offset:3072 sc1 lds\n\t"
        "70:\n\t"
        "ds_read_b32 v72, v93 offset:256\n\t"
        "ds_read_u16 v64, v92 offset:128\n\t"
        "ds_read_u16 v66, v92 offset:144\n\t"
        "ds_read_u16 v68, v92 offset:160\n\t"
        "ds_read_u16 v70, v92 offset:176\n\t"
        "ds_read_b32 v74, v93 offset:512\n\t"
        "s_cmp_lg_u32 s52, 0\n\t"
        "s_cbranch_scc1 14f\n\t"
        "s_mul_i32 s51, s54, 1024\n\t"
        "v_and_b32 v203, 3, v196\n\t"
        "v_and_b32 v204, -4, v196\n\t"
        "v_add_u32 v203, -1, v203\n\t"
        "v_add3_u32 v204, v204, v197, v191\n\t"
        "v_cmp_lt_u32 vcc, v203, v191\n\t"
        "v_cmp_gt_u32 s[32:33], 4, v191\n\t"
        "v_lshlrev_b32 v204, 3, v204\n\t"
        "v_mov_b64 v[200:201], 0\n\t"
        "v_subrev_u32 v204, s51, v204\n\t"
        "s_and_b64 exec, vcc, s[32:33]\n\t"
        "ds_write_b64 v204, v[200:201] offset:41984\n\t"
        "s_mov_b64 exec, -1\n\t"
        "s_mul_i32 s50, s54, 5120\n\t"
        "v_and_b32 v203, 1, v190\n\t"
        "v_lshl_add_u32 v208, v190, 8, s50\n\t"
        "s_mul_i32 s50, s54, 8960\n\t"
        "s_add_u32 s50, s50, 11200\n\t"
        "v_lshl_or_b32 v208, v203, 7, v208\n\t"
        "v_lshl_add_u32 v209, v190, 5, s50\n\t"
        "v_bfe_u32 v210, v190, 3, 1\n\t"
        "s_lshl_b32 s50, s54, 2\n\t"
        "v_add_u32 v205, 3, v196\n\t"
        "v_or_b32 v210, s50, v210\n\t"
        "s_mul_i32 s50, s54, 240\n\t"
        "v_lshrrev_b32 v201, 2, v197\n\t"
        "v_lshrrev_b32 v200, 2, v205\n\t"
        "v_mov_b32 v202, v191\n\t"
        "v_mov_b32 v207, 0x3f80\n\t"
        "v_cmp_lt_u32 vcc, v202, v200\n\t"
        "s_and_b64 exec, exec, vcc\n\t"
        "s_cbranch_scc0 14f\n\t"
        "13:\n\t"
        "v_add_u32 v203, v201, v202\n\t"
        "v_add_u32 v202, 12, v202\n\t"
        "v_lshlrev_b32 v204, 1, v203\n\t"
        "v_subrev_u32 v203, s50, v203\n\t"
        "ds_write_b16 v204, v208 offset:10240\n\t"
        "v_lshrrev_b32 v205, 1, v203\n\t"
        "v_and_b32 v204, 2, v204\n\t"
        "v_and_or_b32 v204, v203, 4, v204\n\t"
        "v_and_or_b32 v204, v205, 8, v204\n\t"
        "v_xor_b32 v205, v205, v210\n\t"
        "v_lshrrev_b32 v203, 5, v203\n\t"
        "v_bfe_u32 v206, v205, 2, 1\n\t"
        "v_and_b32 v205, 1, v205\n\t"
        "v_mad_u32_u24 v203, v206, 7, v203\n\t"
        "v_lshl_or_b32 v204, v205, 4, v204\n\t"
        "v_mul_u32_u24 v203, 0x280, v203\n\t"
        "v_cmp_lt_u32 vcc, v202, v200\n\t"
        "v_add3_u32 v203, v203, v204, v209\n\t"
        "ds_write_b16 v203, v207\n\t"
        "s_and_b64 exec, exec, vcc\n\t"
        "s_cbranch_scc1 13b\n\t"
        "14:\n\t"
        "s_mov_b64 exec, -1\n\t"
        "v_mov_b64 v[48:49], 0\n\t"
        "v_mov_b64 v[50:51], 0\n\t"
        "v_mov_b64 v[52:53], 0\n\t"
        "v_mov_b64 v[54:55], 0\n\t"
        "v_mov_b64 v[56:57], 0\n\t"
        "v_mov_b64 v[58:59], 0\n\t"
        "s_waitcnt lgkmcnt(0)\n\t"
        "s_barrier\n\t"
        "s_cmp_lt_u32 s53, 4\n\t"
        "s_cbranch_scc1 71f\n\t"
        "s_setprio 1\n\t"
        "ds_read_u16 v80, v92\n\t"
        "ds_read_u16 v82, v92 offset:16\n\t"
        "ds_read_u16 v84, v92 offset:32\n\t"
        "ds_read_u16 v86, v92 offset:48\n\t"
        "ds_read_b32 v88, v93\n\t"
        "s_cmp_gt_u32 s28, 0\n\t"
        "s_cselect_b32 s26, 320000, 0\n\t"
        "s_mov_b32 m0, s30\n\t"
        "s_waitcnt lgkmcnt(0)\n\t"
        "buffer_load_dwordx4 v[88:89], s[24:27], 0 idxen offen lds\n\t"
        "s_mov_b32 m0, s29\n\t"
        "s_nop 0\n\t"
        "buffer_load_dwordx4 v[80:81], s[20:23], s76 idxen offen sc1 lds\n\t"
        "buffer_load_dwordx4 v[82:83], s[20:23], s76 idxen offen offset:1024 sc1 lds\n\t"
        "buffer_load_dwordx4 v[84:85], s[20:23], s76 idxen offen offset:2048 sc1 lds\n\t"
        "buffer_load_dwordx4 v[86:87], s[20:23], s76 idxen offen offset:3072 sc1 lds\n\t"
        "71:\n\t"
        "ds_read_u16 v90, v94\n\t"
        "ds_read_u16 v5, v94 offset:32\n\t"
        "ds_read_b64 v[160:161], v102\n\t"
        "ds_read_u16 v80, v92 offset:256\n\t"
        "ds_read_u16 v82, v92 offset:272\n\t"
        "ds_read_u16 v84, v92 offset:288\n\t"
        "ds_read_u16 v86, v92 offset:304\n\t"
        "ds_read_b32 v88, v93 offset:768\n\t"
        "s_cmp_gt_u32 s28, 1\n\t"
        "s_cselect_b32 s26, 320000, 0\n\t"
        "s_add_u32 m0, s30, 1024\n\t"
        "s_nop 0\n\t"
        "buffer_load_dwordx4 v[72:73], s[24:27], 0 idxen offen lds\n\t"
        "s_add_u32 m0, s29, 4096\n\t"
        "s_nop 0\n\t"
        "buffer_load_dwordx4 v[64:65], s[20:23], s76 idxen offen sc1 lds\n\t"
        "buffer_load_dwordx4 v[66:67], s[20:23], s76 idxen offen offset:1024 sc1 lds\n\t"
        "buffer_load_dwordx4 v[68:69], s[20:23], s76 idxen offen offset:2048 sc1 lds\n\t"
        "buffer_load_dwordx4 v[70:71], s[20:23], s76 idxen offen offset:3072 sc1 lds\n\t"
        "v_mov_b64 v[78:79], v[74:75]\n\t"
        "v_mov_b64 v[66:67], 0\n\t"
        "v_mov_b64 v[70:71], 0\n\t"
        "s_mov_b32 s58, s28\n\t"
        "s_cmp_eq_u32 s28, 0\n\t"
        "s_cbranch_scc1 3f\n\t"
        "s_cmp_gt_u32 s28, 2\n\t"
        "s_cselect_b32 s26, 320000, 0\n\t"
        "s_add_u32 m0, s30, 2048\n\t"
        "s_waitcnt lgkmcnt(0)\n\t"
        "v_xor_b32 v91, v90, v101\n\t"
        "v_mov_b32 v90, v5\n\t"
        "v_mov_b64 v[32:33], v[78:79]\n\t"
        "ds_read_b64_tr_b16 v[64:65], v91\n\t"
        "ds_read_b64_tr_b16 v[68:69], v91 offset:64\n\t"
        "s_waitcnt vmcnt(5)\n\t"
        "ds_read_b128 v[60:63], v100\n\t"
        "ds_read_b128 v[72:75], v96\n\t"
        "ds_read_b128 v[76:79], v97\n\t"
        "s_waitcnt lgkmcnt(0)\n\t"
        "v_mfma_f32_32x32x16_bf16 v[0:15], v[72:75], v[152:155], 0\n\t"
        "v_mfma_f32_32x32x16_bf16 v[0:15], v[76:79], v[156:159], v[0:15]\n\t"
        "ds_read_b128 v[72:75], v98\n\t"
        "ds_read_b128 v[76:79], v99\n\t"
        "v_mfma_f32_32x32x16_bf16 v[0:15], v[160:163], v[64:67], v[0:15]\n\t"
        "buffer_load_dwordx4 v[32:33], s[24:27], 0 idxen offen lds\n\t"
        "v_mfma_f32_32x32x16_bf16 v[0:15], v[60:63], v[136:139], v[0:15]\n\t"
        "s_cmp_gt_u32 s28, 3\n\t"
        "s_cselect_b32 s26, 320000, 0\n\t"
        "s_mov_b32 m0, s29\n\t"
        "s_nop 0\n\t"
        "s_nop 11\n\t"
        "1:\n\t"
        "s_waitcnt lgkmcnt(0)\n\t"
        "v_xor_b32 v91, v90, v101\n\t"
        "v_mfma_f32_32x32x16_bf16 v[16:31], v[72:75], v[152:155], 0\n\t"
        "buffer_load_dwordx4 v[80:81], s[20:23], s76 idxen offen sc1 lds\n\t"
        "v_exp_f32 v32, v0\n\t"
        "v_exp_f32 v33, v1\n\t"
        "v_exp_f32 v34, v2\n\t"
        "v_mfma_f32_32x32x16_bf16 v[16:31], v[76:79], v[156:159], v[16:31]\n\t"
        "buffer_load_dwordx4 v[82:83], s[20:23], s76 idxen offen offset:1024 sc1 lds\n\t"
        "v_exp_f32 v35, v3\n\t"
        "v_exp_f32 v36, v4\n\t"
        "v_exp_f32 v37, v5\n\t"
        "v_mfma_f32_32x32x16_bf16 v[16:31], v[160:163], v[68:71], v[16:31]\n\t"
        "buffer_load_dwordx4 v[84:85], s[20:23], s76 idxen offen offset:2048 sc1 lds\n\t"
        "ds_read_b64 v[160:161], v102 offset:512\n\t"
        "v_exp_f32 v38, v6\n\t"
        "v_exp_f32 v39, v7\n\t"
        "v_exp_f32 v40, v8\n\t"
        "v_mfma_f32_32x32x16_bf16 v[16:31], v[60:63], v[140:143], v[16:31]\n\t"
        "buffer_load_dwordx4 v[86:87], s[20:23], s76 idxen offen offset:3072 sc1 lds\n\t"
        "v_exp_f32 v41, v9\n\t"
        "v_exp_f32 v42, v10\n\t"
        "v_exp_f32 v43, v11\n\t"
        "v_exp_f32 v44, v12\n\t"
        "v_exp_f32 v45, v13\n\t"
        "v_exp_f32 v46, v14\n\t"
        "v_exp_f32 v47, v15\n\t"
        "v_pk_add_f32 v[32:33], v[32:33], 1.0 op_sel_hi:[1,0]\n\t"
        "s_xor_b32 s30, s30, 2048\n\t"
        "v_pk_add_f32 v[34:35], v[34:35], 1.0 op_sel_hi:[1,0]\n\t"
        "s_add_u32 m0, s30, 1024\n\t"
        "v_pk_add_f32 v[36:37], v[36:37], 1.0 op_sel_hi:[1,0]\n\t"
        "v_pk_add_f32 v[38:39], v[38:39], 1.0 op_sel_hi:[1,0]\n\t"
        "v_pk_add_f32 v[40:41], v[40:41], 1.0 op_sel_hi:[1,0]\n\t"
        "v_pk_add_f32 v[42:43], v[42:43], 1.0 op_sel_hi:[1,0]\n\t"
        "v_pk_add_f32 v[44:45], v[44:45], 1.0 op_sel_hi:[1,0]\n\t"
        "v_pk_add_f32 v[46:47], v[46:47], 1.0 op_sel_hi:[1,0]\n\t"
        "s_nop 0\n\t"
        "buffer_load_dwordx4 v[88:89], s[24:27], 0 idxen offen lds\n\t"
        "s_waitcnt vmcnt(6)\n\t"
        "ds_read_b128 v[60:63], v100 offset:1024\n\t"
        "ds_read_b128 v[72:75], v96 offset:4096\n\t"
        "ds_read_b128 v[76:79], v97 offset:4096\n\t"
        "ds_read_b64_tr_b16 v[64:65], v91\n\t"
        "v_rcp_f32 v32, v32\n\t"
        "v_rcp_f32 v33, v33\n\t"
        "v_rcp_f32 v40, v40\n\t"
        "v_mfma_f32_32x32x16_bf16 v[120:135], v[56:59], v[52:55], v[120:135]\n\t"
        "v_rcp_f32 v41, v41\n\t"
        "v_rcp_f32 v34, v34\n\t"
        "v_rcp_f32 v35, v35\n\t"
        "v_rcp_f32 v42, v42\n\t"
        "v_rcp_f32 v43, v43\n\t"
        "v_rcp_f32 v36, v36\n\t"
        "v_rcp_f32 v37, v37\n\t"
        "v_pk_mul_f32 v[32:33], v[32:33], v[0:1]\n\t"
        "v_pk_mul_f32 v[40:41], v[40:41], v[8:9]\n\t"
        "v_rcp_f32 v44, v44\n\t"
        "v_rcp_f32 v45, v45\n\t"
        "v_pk_fma_f32 v[32:33], v[34:35], v[2:3], v[32:33]\n\t"
        "v_pk_fma_f32 v[40:41], v[42:43], v[10:11], v[40:41]\n\t"
        "v_rcp_f32 v38, v38\n\t"
        "v_rcp_f32 v39, v39\n\t"
        "v_pk_fma_f32 v[32:33], v[36:37], v[4:5], v[32:33]\n\t"
        "v_rcp_f32 v46, v46\n\t"
        "v_rcp_f32 v47, v47\n\t"
        "v_pk_fma_f32 v[40:41], v[44:45], v[12:13], v[40:41]\n\t"
        "v_pk_fma_f32 v[32:33], v[38:39], v[6:7], v[32:33]\n\t"
        "v_xor_b32 v100, 0x800, v100\n\t"
        "v_pk_fma_f32 v[40:41], v[46:47], v[14:15], v[40:41]\n\t"
        "v_cvt_pk_bf16_f32 v48, v32, v33\n\t"
        "v_cvt_pk_bf16_f32 v49, v40, v41\n\t"
        "s_waitcnt lgkmcnt(0)\n\t"
        "v_mfma_f32_32x32x16_bf16 v[0:15], v[72:75], v[152:155], 0\n\t"
        "ds_read_u16 v80, v92 offset:384\n\t"
        "ds_read_u16 v82, v92 offset:400\n\t"
        "v_exp_f32 v32, v16\n\t"
        "v_exp_f32 v33, v17\n\t"
        "v_exp_f32 v34, v18\n\t"
        "v_mfma_f32_32x32x16_bf16 v[0:15], v[76:79], v[156:159], v[0:15]\n\t"
        "ds_read_b128 v[72:75], v98 offset:4096\n\t"
        "ds_read_b128 v[76:79], v99 offset:4096\n\t"
        "v_exp_f32 v35, v19\n\t"
        "v_exp_f32 v36, v20\n\t"
        "v_exp_f32 v37, v21\n\t"
        "v_mfma_f32_32x32x16_bf16 v[0:15], v[160:163], v[64:67], v[0:15]\n\t"
        "ds_read_b64_tr_b16 v[68:69], v91 offset:64\n\t"
        "ds_read_u16 v84, v92 offset:416\n\t"
        "v_exp_f32 v38, v22\n\t"
        "v_exp_f32 v39, v23\n\t"
        "v_exp_f32 v40, v24\n\t"
        "v_mfma_f32_32x32x16_bf16 v[0:15], v[60:63], v[136:139], v[0:15]\n\t"
        "ds_read_u16 v86, v92 offset:432\n\t"
        "ds_read_b32 v88, v93 offset:1024\n\t"
        "v_exp_f32 v41, v25\n\t"
        "v_exp_f32 v42, v26\n\t"
        "v_exp_f32 v43, v27\n\t"
        "v_exp_f32 v44, v28\n\t"
        "v_exp_f32 v45, v29\n\t"
        "v_exp_f32 v46, v30\n\t"
        "v_exp_f32 v47, v31\n\t"
        "v_pk_add_f32 v[32:33], v[32:33], 1.0 op_sel_hi:[1,0]\n\t"
        "s_sub_u32 s28, s28, 1\n\t"
        "v_pk_add_f32 v[34:35], v[34:35], 1.0 op_sel_hi:[1,0]\n\t"
        "s_cmp_gt_u32 s28, 3\n\t"
        "v_pk_add_f32 v[36:37], v[36:37], 1.0 op_sel_hi:[1,0]\n\t"
        "s_cselect_b32 s26, 320000, 0\n\t"
        "v_pk_add_f32 v[38:39], v[38:39], 1.0 op_sel_hi:[1,0]\n\t"
        "s_add_u32 m0, s29, 4096\n\t"
        "v_pk_add_f32 v[40:41], v[40:41], 1.0 op_sel_hi:[1,0]\n\t"
        "v_pk_add_f32 v[42:43], v[42:43], 1.0 op_sel_hi:[1,0]\n\t"
        "v_pk_add_f32 v[44:45], v[44:45], 1.0 op_sel_hi:[1,0]\n\t"
        "v_pk_add_f32 v[46:47], v[46:47], 1.0 op_sel_hi:[1,0]\n\t"
        "s_nop 0\n\t"
        "ds_read_u16 v90, v94 offset:64\n\t"
        "ds_read_b128 v[56:59], v95\n\t"
        "v_rcp_f32 v32, v32\n\t"
        "v_rcp_f32 v33, v33\n\t"
        "v_rcp_f32 v40, v40\n\t"
        "v_rcp_f32 v41, v41\n\t"
        "v_rcp_f32 v34, v34\n\t"
        "v_rcp_f32 v35, v35\n\t"
        "v_rcp_f32 v42, v42\n\t"
        "v_rcp_f32 v43, v43\n\t"
        "v_rcp_f32 v36, v36\n\t"
        "v_rcp_f32 v37, v37\n\t"
        "v_pk_mul_f32 v[32:33], v[32:33], v[16:17]\n\t"
        "v_pk_mul_f32 v[40:41], v[40:41], v[24:25]\n\t"
        "v_rcp_f32 v44, v44\n\t"
        "v_rcp_f32 v45, v45\n\t"
        "v_pk_fma_f32 v[32:33], v[34:35], v[18:19], v[32:33]\n\t"
        "v_pk_fma_f32 v[40:41], v[42:43], v[26:27], v[40:41]\n\t"
        "v_rcp_f32 v38, v38\n\t"
        "v_rcp_f32 v39, v39\n\t"
        "v_pk_fma_f32 v[32:33], v[36:37], v[20:21], v[32:33]\n\t"
        "v_rcp_f32 v46, v46\n\t"
        "v_rcp_f32 v47, v47\n\t"
        "v_pk_fma_f32 v[40:41], v[44:45], v[28:29], v[40:41]\n\t"
        "v_pk_fma_f32 v[32:33], v[38:39], v[22:23], v[32:33]\n\t"
        "v_pk_fma_f32 v[40:41], v[46:47], v[30:31], v[40:41]\n\t"
        "v_cvt_pk_bf16_f32 v52, v32, v33\n\t"
        "s_cmp_eq_u32 s28, 0\n\t"
        "v_cvt_pk_bf16_f32 v53, v40, v41\n\t"
        "s_cbranch_scc1 2f\n\t"
        "s_waitcnt lgkmcnt(0)\n\t"
        "v_xor_b32 v91, v90, v101\n\t"
        "v_mfma_f32_32x32x16_bf16 v[16:31], v[72:75], v[152:155], 0\n\t"
        "buffer_load_dwordx4 v[80:81], s[20:23], s76 idxen offen sc1 lds\n\t"
        "v_exp_f32 v32, v0\n\t"
        "v_exp_f32 v33, v1\n\t"
        "v_exp_f32 v34, v2\n\t"
        "v_mfma_f32_32x32x16_bf16 v[16:31], v[76:79], v[156:159], v[16:31]\n\t"
        "buffer_load_dwordx4 v[82:83], s[20:23], s76 idxen offen offset:1024 sc1 lds\n\t"
        "v_exp_f32 v35, v3\n\t"
        "v_exp_f32 v36, v4\n\t"
        "v_exp_f32 v37, v5\n\t"
        "v_mfma_f32_32x32x16_bf16 v[16:31], v[160:163], v[68:71], v[16:31]\n\t"
        "buffer_load_dwordx4 v[84:85], s[20:23], s76 idxen offen offset:2048 sc1 lds\n\t"
        "ds_read_b64 v[160:161], v102 offset:1024\n\t"
        "v_exp_f32 v38, v6\n\t"
        "v_exp_f32 v39, v7\n\t"
        "v_exp_f32 v40, v8\n\t"
        "v_mfma_f32_32x32x16_bf16 v[16:31], v[60:63], v[140:143], v[16:31]\n\t"
        "buffer_load_dwordx4 v[86:87], s[20:23], s76 idxen offen offset:3072 sc1 lds\n\t"
        "v_exp_f32 v41, v9\n\t"
        "v_exp_f32 v42, v10\n\t"
        "v_exp_f32 v43, v11\n\t"
        "v_exp_f32 v44, v12\n\t"
        "v_exp_f32 v45, v13\n\t"
        "v_exp_f32 v46, v14\n\t"
        "v_exp_f32 v47, v15\n\t"
        "v_pk_add_f32 v[32:33], v[32:33], 1.0 op_sel_hi:[1,0]\n\t"
        "s_xor_b32 m0, s30, 2048\n\t"
        "v_pk_add_f32 v[34:35], v[34:35], 1.0 op_sel_hi:[1,0]\n\t"
        "v_pk_add_f32 v[36:37], v[36:37], 1.0 op_sel_hi:[1,0]\n\t"
        "v_pk_add_f32 v[38:39], v[38:39], 1.0 op_sel_hi:[1,0]\n\t"
        "v_pk_add_f32 v[40:41], v[40:41], 1.0 op_sel_hi:[1,0]\n\t"
        "v_pk_add_f32 v[42:43], v[42:43], 1.0 op_sel_hi:[1,0]\n\t"
        "v_pk_add_f32 v[44:45], v[44:45], 1.0 op_sel_hi:[1,0]\n\t"
        "v_pk_add_f32 v[46:47], v[46:47], 1.0 op_sel_hi:[1,0]\n\t"
        "s_nop 0\n\t"
        "buffer_load_dwordx4 v[88:89], s[24:27], 0 idxen offen lds\n\t"
        "s_waitcnt vmcnt(6)\n\t"
        "ds_read_b128 v[60:63], v100\n\t"
        "ds_read_b128 v[72:75], v96\n\t"
        "ds_read_b128 v[76:79], v97\n\t"
        "ds_read_b64_tr_b16 v[64:65], v91\n\t"
        "v_rcp_f32 v32, v32\n\t"
        "v_rcp_f32 v33, v33\n\t"
        "v_rcp_f32 v40, v40\n\t"
        "v_rcp_f32 v41, v41\n\t"
        "v_rcp_f32 v34, v34\n\t"
        "v_rcp_f32 v35, v35\n\t"
        "v_rcp_f32 v42, v42\n\t"
        "v_rcp_f32 v43, v43\n\t"
        "v_rcp_f32 v36, v36\n\t"
        "v_rcp_f32 v37, v37\n\t"
        "v_pk_mul_f32 v[32:33], v[32:33], v[0:1]\n\t"
        "v_pk_mul_f32 v[40:41], v[40:41], v[8:9]\n\t"
        "v_rcp_f32 v44, v44\n\t"
        "v_rcp_f32 v45, v45\n\t"
        "v_pk_fma_f32 v[32:33], v[34:35], v[2:3], v[32:33]\n\t"
        "v_pk_fma_f32 v[40:41], v[42:43], v[10:11], v[40:41]\n\t"
        "v_rcp_f32 v38, v38\n\t"
        "v_rcp_f32 v39, v39\n\t"
        "v_pk_fma_f32 v[32:33], v[36:37], v[4:5], v[32:33]\n\t"
        "v_rcp_f32 v46, v46\n\t"
        "v_rcp_f32 v47, v47\n\t"
        "v_pk_fma_f32 v[40:41], v[44:45], v[12:13], v[40:41]\n\t"
        "v_pk_fma_f32 v[32:33], v[38:39], v[6:7], v[32:33]\n\t"
        "v_add_u32 v102, v103, v102\n\t"
        "v_pk_fma_f32 v[40:41], v[46:47], v[14:15], v[40:41]\n\t"
        "v_cvt_pk_bf16_f32 v50, v32, v33\n\t"
        "v_cvt_pk_bf16_f32 v51, v40, v41\n\t"
        "s_waitcnt lgkmcnt(0)\n\t"
        "v_mfma_f32_32x32x16_bf16 v[0:15], v[72:75], v[152:155], 0\n\t"
        "ds_read_u16 v80, v92 offset:512\n\t"
        "ds_read_u16 v82, v92 offset:528\n\t"
        "v_exp_f32 v32, v16\n\t"
        "v_exp_f32 v33, v17\n\t"
        "v_exp_f32 v34, v18\n\t"
        "v_mfma_f32_32x32x16_bf16 v[0:15], v[76:79], v[156:159], v[0:15]\n\t"
        "ds_read_b128 v[72:75], v98\n\t"
        "ds_read_b128 v[76:79], v99\n\t"
        "v_exp_f32 v35, v19\n\t"
        "v_exp_f32 v36, v20\n\t"
        "v_exp_f32 v37, v21\n\t"
        "v_mfma_f32_32x32x16_bf16 v[0:15], v[160:163], v[64:67], v[0:15]\n\t"
        "ds_read_b64_tr_b16 v[68:69], v91 offset:64\n\t"
        "ds_read_u16 v84, v92 offset:544\n\t"
        "v_exp_f32 v38, v22\n\t"
        "v_exp_f32 v39, v23\n\t"
        "v_exp_f32 v40, v24\n\t"
        "v_mfma_f32_32x32x16_bf16 v[0:15], v[60:63], v[136:139], v[0:15]\n\t"
        "ds_read_u16 v86, v92 offset:560\n\t"
        "ds_read_b32 v88, v93 offset:1280\n\t"
        "v_exp_f32 v41, v25\n\t"
        "v_exp_f32 v42, v26\n\t"
        "v_exp_f32 v43, v27\n\t"
        "v_exp_f32 v44, v28\n\t"
        "v_exp_f32 v45, v29\n\t"
        "v_exp_f32 v46, v30\n\t"
        "v_exp_f32 v47, v31\n\t"
        "v_pk_add_f32 v[32:33], v[32:33], 1.0 op_sel_hi:[1,0]\n\t"
        "s_sub_u32 s28, s28, 1\n\t"
        "v_pk_add_f32 v[34:35], v[34:35], 1.0 op_sel_hi:[1,0]\n\t"
        "s_cmp_gt_u32 s28, 3\n\t"
        "v_pk_add_f32 v[36:37], v[36:37], 1.0 op_sel_hi:[1,0]\n\t"
        "s_cselect_b32 s26, 320000, 0\n\t"
        "v_pk_add_f32 v[38:39], v[38:39], 1.0 op_sel_hi:[1,0]\n\t"
        "s_add_u32 m0, s29, 0\n\t"
        "v_pk_add_f32 v[40:41], v[40:41], 1.0 op_sel_hi:[1,0]\n\t"
        "v_pk_add_f32 v[42:43], v[42:43], 1.0 op_sel_hi:[1,0]\n\t"
        "v_pk_add_f32 v[44:45], v[44:45], 1.0 op_sel_hi:[1,0]\n\t"
        "v_pk_add_f32 v[46:47], v[46:47], 1.0 op_sel_hi:[1,0]\n\t"
        "s_nop 0\n\t"
        "ds_read_u16 v90, v94 offset:96\n\t"
        "v_rcp_f32 v32, v32\n\t"
        "v_rcp_f32 v33, v33\n\t"
        "v_rcp_f32 v40, v40\n\t"
        "v_mfma_f32_32x32x16_bf16 v[104:119], v[56:59], v[48:51], v[104:119]\n\t"
        "v_rcp_f32 v41, v41\n\t"
        "v_rcp_f32 v34, v34\n\t"
        "v_rcp_f32 v35, v35\n\t"
        "v_rcp_f32 v42, v42\n\t"
        "v_rcp_f32 v43, v43\n\t"
        "v_rcp_f32 v36, v36\n\t"
        "v_rcp_f32 v37, v37\n\t"
        "v_pk_mul_f32 v[32:33], v[32:33], v[16:17]\n\t"
        "v_pk_mul_f32 v[40:41], v[40:41], v[24:25]\n\t"
        "v_rcp_f32 v44, v44\n\t"
        "v_rcp_f32 v45, v45\n\t"
        "v_pk_fma_f32 v[32:33], v[34:35], v[18:19], v[32:33]\n\t"
        "v_pk_fma_f32 v[40:41], v[42:43], v[26:27], v[40:41]\n\t"
        "v_rcp_f32 v38, v38\n\t"
        "v_rcp_f32 v39, v39\n\t"
        "v_pk_fma_f32 v[32:33], v[36:37], v[20:21], v[32:33]\n\t"
        "v_rcp_f32 v46, v46\n\t"
        "v_rcp_f32 v47, v47\n\t"
        "v_pk_fma_f32 v[40:41], v[44:45], v[28:29], v[40:41]\n\t"
        "v_pk_fma_f32 v[32:33], v[38:39], v[22:23], v[32:33]\n\t"
        "v_add_u32 v92, 0x100, v92\n\t"
        "v_pk_fma_f32 v[40:41], v[46:47], v[30:31], v[40:41]\n\t"
        "v_add_u32 v93, 0x200, v93\n\t"
        "v_add_u32 v94, 64, v94\n\t"
        "v_add_u32 v95, 0x280, v95\n\t"
        "v_cvt_pk_bf16_f32 v54, v32, v33\n\t"
        "s_cmp_eq_u32 s28, 0\n\t"
        "v_cvt_pk_bf16_f32 v55, v40, v41\n\t"
        "s_cbranch_scc0 1b\n\t"
        "s_nop 1\n\t"
        "v_mfma_f32_32x32x16_bf16 v[120:135], v[56:59], v[52:55], v[120:135]\n\t"
        "s_branch 3f\n\t"
        "2:\n\t"
        "s_waitcnt lgkmcnt(0)\n\t"
        "s_nop 0\n\t"
        "v_mfma_f32_32x32x16_bf16 v[104:119], v[56:59], v[48:51], v[104:119]\n\t"
        "v_mfma_f32_32x32x16_bf16 v[120:135], v[56:59], v[52:55], v[120:135]\n\t"
        "3:\n\t"
        "s_setprio 0\n\t"
        "s_waitcnt lgkmcnt(0)\n\t"
        "s_waitcnt vmcnt(0) lgkmcnt(0)\n\t"
        "s_mov_b32 m0, s31\n\t"
        "s_nop 15\n\t"
          : "+{v[104:119]}"(agg0), "+{v[120:135]}"(agg1), "={s52}"(more), "={s58}"(nlast), "+{s[32:47]}"(sc)
          : "{v[136:151]}"(w3), "{v[152:159]}"(ident), "{v[180:187]}"(en), "{v188}"(cbase), "{v244}"(tid0),
            "{s[20:23]}"(sp), "{s[24:25]}"((u32x2){sf[0], sf[1]}), "{s27}"(sf[3]), "{s53}"(w), "{s59}"(m0)
          : "v0", "v1", "v2", "v3", "v4", "v5", "v6", "v7", "v8", "v9", "v10", "v11", "v12", "v13", "v14", "v15", "v16", "v17", "v18", "v19", "v20", "v21", "v22", "v23", "v24", "v25", "v26", "v27", "v28", "v29", "v30", "v31", "v32", "v33", "v34", "v35", "v36", "v37", "v38", "v39", "v40", "v41", "v42", "v43", "v44", "v45", "v46", "v47", "v48", "v49", "v50", "v51", "v52", "v53", "v54", "v55", "v56", "v57", "v58", "v59", "v60", "v61", "v62", "v63", "v64", "v65", "v66", "v67", "v68", "v69", "v70", "v71", "v72", "v73", "v74", "v75", "v76", "v77", "v78", "v79", "v80", "v81", "v82", "v83", "v84", "v85", "v86", "v87", "v88", "v89", "v90", "v91", "v92", "v93", "v94", "v95", "v96", "v97", "v98", "v99", "v100", "v101", "v102", "v103", "v160", "v161", "v162", "v163", "v189", "v190", "v191", "v192", "v193", "v194", "v195", "v196", "v197", "v198", "v199", "v200", "v201", "v202", "v203", "v204", "v205", "v206", "v207", "v208", "v209", "v210", "v211", "v245", "v246", "v247", "s26", "s28", "s29", "s30", "s31", "s48", "s49", "s50", "s51", "s54", "s55", "s56", "s57", "s62", "s63", "s76", "s77", "vcc", "scc", "memory");
      if (mode == 1) { if (++cur == CH * CAP / 512) { mode = 2; cur = 0; curo = 0; } }
      else curo += 512;
    }
    asm volatile(
        "s_mov_b32 s31, m0\n\t"
        "s_lshr_b32 s54, s53, 2\n\t"
        "s_lshr_b32 s56, s53, 1\n\t"
        "s_and_b32 s62, s53, 1\n\t"
        "s_and_b32 s63, s56, 1\n\t"
        "s_xor_b32 s55, s63, s54\n\t"
        "s_xor_b32 s57, s62, s63\n\t"
        "s_lshl_b32 s76, s62, 7\n\t"
        "s_lshl_b32 s29, s53, 13\n\t"
        "s_lshl_b32 s30, s53, 12\n\t"
        "s_add_u32 s29, s29, 98304\n\t"
        "s_add_u32 s30, s30, 65536\n\t"
        "v_and_b32 v245, 63, v244\n\t"
        "v_lshlrev_b32 v245, 4, v245\n\t"
        "v_xor_b32 v81, 32, v248\n\t"
        "v_xor_b32 v82, 64, v248\n\t"
        "v_xor_b32 v83, 96, v248\n\t"
        "v_xor_b32 v84, 128, v248\n\t"
        "v_xor_b32 v85, 160, v248\n\t"
        "v_xor_b32 v86, 192, v248\n\t"
        "v_xor_b32 v87, 224, v248\n\t"
        "s_lshl_b32 s51, s53, 12\n\t"
        "s_add_u32 s51, s51, 65536\n\t"
        "ds_read_b128 v[0:3], v248\n\t"
        "ds_read_b128 v[4:7], v81\n\t"
        "ds_read_b128 v[8:11], v82\n\t"
        "ds_read_b128 v[12:15], v83\n\t"
        "ds_read_b128 v[16:19], v84\n\t"
        "ds_read_b128 v[20:23], v85\n\t"
        "ds_read_b128 v[24:27], v86\n\t"
        "ds_read_b128 v[28:31], v87\n\t"
        "v_add_u32 v64, s51, v250\n\t"
        "s_and_b32 s50, s58, 1\n\t"
        "s_lshl_b32 s50, s50, 12\n\t"
        "v_xor_b32 v65, 16, v64\n\t"
        "s_add_u32 s50, s50, s29\n\t"
        "s_xor_b32 s51, s50, 4096\n\t"
        "v_xor_b32 v66, 32, v64\n\t"
        "v_add_u32 v70, s50, v245\n\t"
        "v_add_u32 v71, s51, v245\n\t"
        "s_waitcnt vmcnt(0)\n\t"
        "s_waitcnt lgkmcnt(7)\n\t"
        "v_mfma_f32_32x32x16_bf16 v[164:179], v[212:215], v[0:3], 0\n\t"
        "v_cvt_pk_bf16_f32 v32, v104, v105\n\t"
        "v_cvt_pk_bf16_f32 v33, v106, v107\n\t"
        "ds_write_b64 v64, v[32:33]\n\t"
        "s_waitcnt lgkmcnt(7)\n\t"
        "v_mfma_f32_32x32x16_bf16 v[164:179], v[216:219], v[4:7], v[164:179]\n\t"
        "v_cvt_pk_bf16_f32 v34, v108, v109\n\t"
        "v_cvt_pk_bf16_f32 v35, v110, v111\n\t"
        "ds_write_b64 v65, v[34:35]\n\t"
        "s_waitcnt lgkmcnt(7)\n\t"
        "v_mfma_f32_32x32x16_bf16 v[164:179], v[220:223], v[8:11], v[164:179]\n\t"
        "v_cvt_pk_bf16_f32 v36, v112, v113\n\t"
        "v_cvt_pk_bf16_f32 v37, v114, v115\n\t"
        "ds_write_b64 v66, v[36:37]\n\t"
        "s_waitcnt lgkmcnt(7)\n\t"
        "v_mfma_f32_32x32x16_bf16 v[164:179], v[224:227], v[12:15], v[164:179]\n\t"
        "v_cvt_pk_bf16_f32 v38, v120, v121\n\t"
        "v_cvt_pk_bf16_f32 v39, v122, v123\n\t"
        "ds_write_b64 v64, v[38:39] offset:2048\n\t"
        "s_waitcnt lgkmcnt(7)\n\t"
        "v_mfma_f32_32x32x16_bf16 v[164:179], v[228:231], v[16:19], v[164:179]\n\t"
        "v_cvt_pk_bf16_f32 v40, v124, v125\n\t"
        "v_cvt_pk_bf16_f32 v41, v126, v127\n\t"
        "ds_write_b64 v65, v[40:41] offset:2048\n\t"
        "s_waitcnt lgkmcnt(7)\n\t"
        "v_mfma_f32_32x32x16_bf16 v[164:179], v[232:235], v[20:23], v[164:179]\n\t"
        "v_cvt_pk_bf16_f32 v42, v128, v129\n\t"
        "v_cvt_pk_bf16_f32 v43, v130, v131\n\t"
        "ds_write_b64 v66, v[42:43] offset:2048\n\t"
        "s_waitcnt lgkmcnt(7)\n\t"
        "v_mfma_f32_32x32x16_bf16 v[164:179], v[236:239], v[24:27], v[164:179]\n\t"
        "ds_read_b128 v[180:183], v70\n\t"
        "s_lshl_b32 s50, s54, 14\n\t"
        "s_add_u32 s50, s50, 65536\n\t"
        "v_add_u32 v67, s50, v251\n\t"
        "s_mul_i32 s50, s54, 5120\n\t"
        "s_lshl_b32 s51, s63, 6\n\t"
        "s_add_u32 s51, s51, s76\n\t"
        "s_add_u32 s50, s50, s51\n\t"
        "v_xor_b32 v68, 0x110, v67\n\t"
        "v_add_u32 v69, s50, v252\n\t"
        "s_waitcnt lgkmcnt(7)\n\t"
        "v_mfma_f32_32x32x16_bf16 v[164:179], v[240:243], v[28:31], v[164:179]\n\t"
        "ds_read_b128 v[184:187], v70 offset:1024\n\t"
        "ds_read_b128 v[188:191], v70 offset:2048\n\t"
        "ds_read_b128 v[192:195], v70 offset:3072\n\t"
        "ds_read_b128 v[196:199], v71\n\t"
        "ds_read_b128 v[200:203], v71 offset:1024\n\t"
        "ds_read_b128 v[204:207], v71 offset:2048\n\t"
        "ds_read_b128 v[208:211], v71 offset:3072\n\t"
        "s_waitcnt lgkmcnt(0)\n\t"
        "s_barrier\n\t"
        "ds_read_b64_tr_b16 v[16:17], v67\n\t"
        "ds_read_b64_tr_b16 v[18:19], v68\n\t"
        "ds_read_b64_tr_b16 v[20:21], v67 offset:8192\n\t"
        "ds_read_b64_tr_b16 v[22:23], v68 offset:8192\n\t"
        "ds_read_b64_tr_b16 v[24:25], v67 offset:1024\n\t"
        "ds_read_b64_tr_b16 v[26:27], v68 offset:1024\n\t"
        "ds_read_b64_tr_b16 v[28:29], v67 offset:9216\n\t"
        "ds_read_b64_tr_b16 v[30:31], v68 offset:9216\n\t"
        "ds_read_b64_tr_b16 v[32:33], v67 offset:2048\n\t"
        "ds_read_b64_tr_b16 v[34:35], v68 offset:2048\n\t"
        "ds_read_b64_tr_b16 v[36:37], v67 offset:10240\n\t"
        "ds_read_b64_tr_b16 v[38:39], v68 offset:10240\n\t"
        "ds_read_b64_tr_b16 v[40:41], v67 offset:3072\n\t"
        "ds_read_b64_tr_b16 v[42:43], v68 offset:3072\n\t"
        "s_waitcnt lgkmcnt(12)\n\t"
        "v_mfma_f32_32x32x16_bf16 v[0:15], v[180:183], v[16:19], 0\n\t"
        "ds_read_b64_tr_b16 v[44:45], v67 offset:11264\n\t"
        "ds_read_b64_tr_b16 v[46:47], v68 offset:11264\n\t"
        "s_waitcnt lgkmcnt(12)\n\t"
        "v_mfma_f32_32x32x16_bf16 v[0:15], v[180:183], v[20:23], v[0:15]\n\t"
        "ds_read_b64_tr_b16 v[48:49], v67 offset:4096\n\t"
        "ds_read_b64_tr_b16 v[50:51], v68 offset:4096\n\t"
        "s_waitcnt lgkmcnt(12)\n\t"
        "v_mfma_f32_32x32x16_bf16 v[0:15], v[184:187], v[24:27], v[0:15]\n\t"
        "ds_read_b64_tr_b16 v[52:53], v67 offset:12288\n\t"
        "ds_read_b64_tr_b16 v[54:55], v68 offset:12288\n\t"
        "s_waitcnt lgkmcnt(12)\n\t"
        "v_mfma_f32_32x32x16_bf16 v[0:15], v[184:187], v[28:31], v[0:15]\n\t"
        "ds_read_b64_tr_b16 v[56:57], v67 offset:5120\n\t"
        "ds_read_b64_tr_b16 v[58:59], v68 offset:5120\n\t"
        "s_waitcnt lgkmcnt(12)\n\t"
        "v_mfma_f32_32x32x16_bf16 v[0:15], v[188:191], v[32:35], v[0:15]\n\t"
        "ds_read_b64_tr_b16 v[60:61], v67 offset:13312\n\t"
        "ds_read_b64_tr_b16 v[62:63], v68 offset:13312\n\t"
        "s_waitcnt lgkmcnt(12)\n\t"
        "v_mfma_f32_32x32x16_bf16 v[0:15], v[188:191], v[36:39], v[0:15]\n\t"
        "ds_read_b64_tr_b16 v[16:17], v67 offset:6144\n\t"
        "ds_read_b64_tr_b16 v[18:19], v68 offset:6144\n\t"
        "s_waitcnt lgkmcnt(12)\n\t"
        "v_mfma_f32_32x32x16_bf16 v[0:15], v[192:195], v[40:43], v[0:15]\n\t"
        "ds_read_b64_tr_b16 v[20:21], v67 offset:14336\n\t"
        "ds_read_b64_tr_b16 v[22:23], v68 offset:14336\n\t"
        "s_waitcnt lgkmcnt(12)\n\t"
        "v_mfma_f32_32x32x16_bf16 v[0:15], v[192:195], v[44:47], v[0:15]\n\t"
        "ds_read_b64_tr_b16 v[24:25], v67 offset:7168\n\t"
        "ds_read_b64_tr_b16 v[26:27], v68 offset:7168\n\t"
        "s_waitcnt lgkmcnt(12)\n\t"
        "v_mfma_f32_32x32x16_bf16 v[0:15], v[196:199], v[48:51], v[0:15]\n\t"
        "ds_read_b64_tr_b16 v[28:29], v67 offset:15360\n\t"
        "ds_read_b64_tr_b16 v[30:31], v68 offset:15360\n\t"
        "s_waitcnt lgkmcnt(12)\n\t"
        "v_mfma_f32_32x32x16_bf16 v[0:15], v[196:199], v[52:55], v[0:15]\n\t"
        "s_waitcnt lgkmcnt(10)\n\t"
        "v_mfma_f32_32x32x16_bf16 v[0:15], v[200:203], v[56:59], v[0:15]\n\t"
        "s_waitcnt lgkmcnt(8)\n\t"
        "v_mfma_f32_32x32x16_bf16 v[0:15], v[200:203], v[60:63], v[0:15]\n\t"
        "s_waitcnt lgkmcnt(6)\n\t"
        "v_mfma_f32_32x32x16_bf16 v[0:15], v[204:207], v[16:19], v[0:15]\n\t"
        "s_waitcnt lgkmcnt(4)\n\t"
        "v_mfma_f32_32x32x16_bf16 v[0:15], v[204:207], v[20:23], v[0:15]\n\t"
        "s_waitcnt lgkmcnt(2)\n\t"
        "v_mfma_f32_32x32x16_bf16 v[0:15], v[208:211], v[24:27], v[0:15]\n\t"
        "s_waitcnt lgkmcnt(0)\n\t"
        "v_mfma_f32_32x32x16_bf16 v[0:15], v[208:211], v[28:31], v[0:15]\n\t"
        "s_mov_b32 s50, 0xbfb8aa3b\n\t"
        "s_mov_b32 s32, 0xbf317218\n\t"
        "s_mov_b32 s33, 0xbf317218\n\t"
        "s_nop 8\n\t"
        "s_mov_b32 exec_lo, 0xfffff\n\t"
        "s_mov_b32 exec_hi, 0xfffff\n\t"
        "v_fmac_f32 v0, s50, v164\n\t"
        "v_fmac_f32 v1, s50, v165\n\t"
        "v_fmac_f32 v2, s50, v166\n\t"
        "v_fmac_f32 v3, s50, v167\n\t"
        "v_fmac_f32 v4, s50, v168\n\t"
        "v_fmac_f32 v5, s50, v169\n\t"
        "v_fmac_f32 v6, s50, v170\n\t"
        "v_fmac_f32 v7, s50, v171\n\t"
        "v_fmac_f32 v8, s50, v172\n\t"
        "v_fmac_f32 v9, s50, v173\n\t"
        "v_fmac_f32 v10, s50, v174\n\t"
        "v_fmac_f32 v11, s50, v175\n\t"
        "v_fmac_f32 v12, s50, v176\n\t"
        "v_fmac_f32 v13, s50, v177\n\t"
        "v_fmac_f32 v14, s50, v178\n\t"
        "v_fmac_f32 v15, s50, v179\n\t"
        "v_exp_f32 v16, v0\n\t"
        "v_exp_f32 v17, v1\n\t"
        "v_exp_f32 v18, v2\n\t"
        "v_exp_f32 v19, v3\n\t"
        "v_exp_f32 v20, v4\n\t"
        "v_exp_f32 v21, v5\n\t"
        "v_exp_f32 v22, v6\n\t"
        "v_exp_f32 v23, v7\n\t"
        "v_exp_f32 v24, v8\n\t"
        "v_exp_f32 v25, v9\n\t"
        "v_exp_f32 v26, v10\n\t"
        "v_exp_f32 v27, v11\n\t"
        "v_exp_f32 v28, v12\n\t"
        "v_exp_f32 v29, v13\n\t"
        "v_exp_f32 v30, v14\n\t"
        "v_exp_f32 v31, v15\n\t"
        "v_pk_add_f32 v[16:17], v[16:17], 1.0 op_sel_hi:[1,0]\n\t"
        "v_pk_add_f32 v[18:19], v[18:19], 1.0 op_sel_hi:[1,0]\n\t"
        "v_pk_add_f32 v[20:21], v[20:21], 1.0 op_sel_hi:[1,0]\n\t"
        "v_pk_add_f32 v[22:23], v[22:23], 1.0 op_sel_hi:[1,0]\n\t"
        "v_pk_add_f32 v[24:25], v[24:25], 1.0 op_sel_hi:[1,0]\n\t"
        "v_pk_add_f32 v[26:27], v[26:27], 1.0 op_sel_hi:[1,0]\n\t"
        "v_pk_add_f32 v[28:29], v[28:29], 1.0 op_sel_hi:[1,0]\n\t"
        "v_pk_add_f32 v[30:31], v[30:31], 1.0 op_sel_hi:[1,0]\n\t"
        "v_rcp_f32 v16, v16\n\t"
        "v_rcp_f32 v17, v17\n\t"
        "v_rcp_f32 v18, v18\n\t"
        "v_rcp_f32 v19, v19\n\t"
        "v_rcp_f32 v20, v20\n\t"
        "v_rcp_f32 v21, v21\n\t"
        "v_rcp_f32 v22, v22\n\t"
        "v_rcp_f32 v23, v23\n\t"
        "v_rcp_f32 v24, v24\n\t"
        "v_rcp_f32 v25, v25\n\t"
        "v_rcp_f32 v26, v26\n\t"
        "v_rcp_f32 v27, v27\n\t"
        "v_rcp_f32 v28, v28\n\t"
        "v_rcp_f32 v29, v29\n\t"
        "v_rcp_f32 v30, v30\n\t"
        "v_rcp_f32 v31, v31\n\t"
        "v_pk_mul_f32 v[0:1], v[0:1], s[32:33]\n\t"
        "v_pk_mul_f32 v[2:3], v[2:3], s[32:33]\n\t"
        "v_pk_mul_f32 v[4:5], v[4:5], s[32:33]\n\t"
        "v_pk_mul_f32 v[6:7], v[6:7], s[32:33]\n\t"
        "v_pk_mul_f32 v[8:9], v[8:9], s[32:33]\n\t"
        "v_pk_mul_f32 v[10:11], v[10:11], s[32:33]\n\t"
        "v_pk_mul_f32 v[12:13], v[12:13], s[32:33]\n\t"
        "v_pk_mul_f32 v[14:15], v[14:15], s[32:33]\n\t"
        "v_pk_mul_f32 v[0:1], v[0:1], v[16:17]\n\t"
        "v_pk_mul_f32 v[2:3], v[2:3], v[18:19]\n\t"
        "v_pk_mul_f32 v[4:5], v[4:5], v[20:21]\n\t"
        "v_pk_mul_f32 v[6:7], v[6:7], v[22:23]\n\t"
        "v_pk_mul_f32 v[8:9], v[8:9], v[24:25]\n\t"
        "v_pk_mul_f32 v[10:11], v[10:11], v[26:27]\n\t"
        "v_pk_mul_f32 v[12:13], v[12:13], v[28:29]\n\t"
        "v_pk_mul_f32 v[14:15], v[14:15], v[30:31]\n\t"
        "v_cvt_pk_bf16_f32 v32, v0, v1\n\t"
        "v_cvt_pk_bf16_f32 v33, v2, v3\n\t"
        "v_cvt_pk_bf16_f32 v34, v4, v5\n\t"
        "v_cvt_pk_bf16_f32 v35, v6, v7\n\t"
        "v_cvt_pk_bf16_f32 v36, v8, v9\n\t"
        "v_cvt_pk_bf16_f32 v37, v10, v11\n\t"
        "v_cvt_pk_bf16_f32 v38, v12, v13\n\t"
        "v_cvt_pk_bf16_f32 v39, v14, v15\n\t"
        "buffer_store_dwordx4 v[32:35], v69, s[72:75], s59 offen\n\t"
        "buffer_store_dwordx4 v[36:39], v69, s[72:75], s59 offen offset:16\n\t"
        "s_mov_b64 exec, -1\n\t"
        "s_nop 1\n\t"
        : "+{v[104:119]}"(agg0), "+{v[120:135]}"(agg1)
        : "{v[212:227]}"(wua), "{v[228:243]}"(wub), "{v[248:255]}"(epi), "{v244}"(tid0), "{s53}"(w), "{s58}"(nlast), "{s59}"(m0),
          "{s[72:75]}"(make_srd(ka->out, 0, bucket_start(bb + 1) * 256))
        : "v0", "v1", "v2", "v3", "v4", "v5", "v6", "v7", "v8", "v9", "v10", "v11", "v12", "v13", "v14", "v15", "v16", "v17", "v18", "v19", "v20", "v21", "v22", "v23", "v24", "v25", "v26", "v27", "v28", "v29", "v30", "v31", "v32", "v33", "v34", "v35", "v36", "v37", "v38", "v39", "v40", "v41", "v42", "v43", "v44", "v45", "v46", "v47", "v48", "v49", "v50", "v51", "v52", "v53", "v54", "v55", "v56", "v57", "v58", "v59", "v60", "v61", "v62", "v63", "v64", "v65", "v66", "v67", "v68", "v69", "v70", "v71", "v72", "v73", "v74", "v75", "v76", "v77", "v78", "v79", "v80", "v81", "v82", "v83", "v84", "v85", "v86", "v87", "v88", "v89", "v90", "v91", "v92", "v93", "v94", "v95", "v96", "v97", "v98", "v99", "v100", "v101", "v102", "v103", "v160", "v161", "v162", "v163", "v164", "v165", "v166", "v167", "v168", "v169", "v170", "v171", "v172", "v173", "v174", "v175", "v176", "v177", "v178", "v179", "v180", "v181", "v182", "v183", "v184", "v185", "v186", "v187", "v188", "v189", "v190", "v191", "v192", "v193", "v194", "v195", "v196", "v197", "v198", "v199", "v200", "v201", "v202", "v203", "v204", "v205", "v206", "v207", "v208", "v209", "v210", "v211", "v245", "v246", "v247", "s28", "s29", "s30", "s31", "s48", "s49", "s50", "s51", "s54", "s55", "s56", "s57", "s62", "s63", "s76", "s77", "s32", "s33", "s34", "s35", "s36", "s37", "s38", "s39", "s40", "s41", "s42", "s43", "s44", "s45", "s46", "s47", "vcc", "scc", "memory");
  }

}
}

extern "C" void kernel_launch(void* const* d_in, const int* in_sizes, int n_in, void* d_out, int out_size, void* d_ws, size_t ws_size,
                              hipStream_t stream) {
  Args a;
  a.x = (const u16*)d_in[0]; a.ea = (const u16*)d_in[1]; a.src = (const int*)d_in[2]; a.dst = (const int*)d_in[3];
  a.wmsg = (const u16*)d_in[4]; a.wupd = (const u16*)d_in[5]; a.ws = (char*)d_ws; a.out = (u16*)d_out;
  mpnn_fused<<<NB, 512, 0, stream>>>(a);
}
```
